```python
import torch

def _softmax(x, axis=-1):
    return torch.softmax(x, dim=axis)
def _gelu(x):
    return torch.nn.functional.gelu(x, approximate="tanh")
def _silu(x):
    return torch.nn.functional.silu(x)
def _sigmoid(x):
    return torch.sigmoid(x)
def _layernorm(x, axis=-1, eps=1e-5):
    m = x.mean(dim=axis, keepdim=True)
    v = x.var(dim=axis, keepdim=True, unbiased=False)
    return (x - m) / torch.sqrt(v + eps)
def _scatter_add(tgt, idx, src, axis=0):
    return tgt.clone().index_add_(axis, idx, src)
def _segment_sum(src, seg, n):
    out = torch.zeros((n,) + src.shape[1:], dtype=src.dtype)
    return out.index_add_(0, seg, src)
def _pad(x, pw):
    flat = tuple(v for pair in reversed(pw) for v in pair)
    return torch.nn.functional.pad(x, flat)

kernel_name = 'recurrent_scan__sliding_block__glu_gated_glu__prompt'

def setup_inputs():

    _w = (lambda a: (lambda t: t.to(torch.bfloat16) if t.is_floating_point() else t)(torch.as_tensor(a)))
    def _A_setup():
        B, S, D = 1, 2048, 576
        return {
            "x": _w((lambda *s: torch.randn(*s, dtype=torch.bfloat16) * 0.05)(B, S, D)),
            "wqkv": _w((lambda *s: torch.randn(*s, dtype=torch.bfloat16) * 0.05)(D, 3 * D)),
            "wo": _w((lambda *s: torch.randn(*s, dtype=torch.bfloat16) * 0.05)(D, D)),
        }
    _A_in = _A_setup()

    def _B_setup():
        B, S, D, F = 1, 2048, 576, 4608
        return {
            "x": _w((lambda *s: torch.randn(*s, dtype=torch.bfloat16) * 0.05)(B, S, D)),
            "w_gate": _w((lambda *s: torch.randn(*s, dtype=torch.bfloat16) * 0.05)(D, F)),
            "w_up": _w((lambda *s: torch.randn(*s, dtype=torch.bfloat16) * 0.05)(D, F)),
            "w_down": _w((lambda *s: torch.randn(*s, dtype=torch.bfloat16) * 0.05)(F, D)),
        }
    _B_in = _B_setup()

    return {
        "A_x": _A_in["x"],
        "A_wqkv": _A_in["wqkv"],
        "A_wo": _A_in["wo"],
        "B_x": _B_in["x"],
        "B_w_gate": _B_in["w_gate"],
        "B_w_up": _B_in["w_up"],
        "B_w_down": _B_in["w_down"],
    }

def reference(A_x, A_wqkv, A_wo, B_x, B_w_gate, B_w_up, B_w_down):
    def _A_ref(x, wqkv, wo):
        B, S, D = x.shape
        H = 32; Dh = D // H; W = 256
        qkv = (lambda x, *d: x.permute(*d))((x @ wqkv).reshape(B, S, 3, H, Dh), 2, 0, 3, 1, 4)
        q, k, v = qkv[0], qkv[1], qkv[2]
        scores = torch.einsum("bhqd,bhkd->bhqk", q, k) * (Dh ** -0.5)
        idx = torch.arange(S)
        mask = (idx[:, None] - idx[None, :] >= 0) & (idx[:, None] - idx[None, :] < W)
        scores = torch.where(mask[None, None], scores, -1e9)
        p = _softmax(scores, axis=-1)
        o = torch.einsum("bhqk,bhkd->bhqd", p, v)
        return (lambda x, *d: x.permute(*d))(o, 0, 2, 1, 3).reshape(B, S, D) @ wo

    def _B_ref(x, w_gate, w_up, w_down):
        g = torch.einsum("bsd,df->bsf", x, w_gate)
        u = torch.einsum("bsd,df->bsf", x, w_up)
        h = _sigmoid(g) * u
        return torch.einsum("bsf,fd->bsd", h, w_down)

    x = A_x
    xn = _layernorm(x, axis=-1)
    s = _A_ref(xn, A_wqkv, A_wo)
    g = _silu(_B_ref(xn, B_w_gate, B_w_up, B_w_down))
    return x + s * g
```

```cpp
#include <hip/hip_runtime.h>
#include <hip/hip_bf16.h>
#include <stdint.h>

typedef unsigned short u16;
__device__ __forceinline__ float bf2f(u16 v){ return __uint_as_float(((unsigned)v)<<16); }
__device__ __forceinline__ u16 f2bf(float f){ unsigned u=__float_as_uint(f); u += 0x7fffu + ((u>>16)&1u); return (u16)(u>>16); }
__device__ __forceinline__ float rbf(float f){ return bf2f(f2bf(f)); }

constexpr int S=2048, D=576, F=4608, H=32, DH=18, W=256;

__global__ void ln_kernel(const u16* x, u16* xn){
  int row = blockIdx.x; int lane = threadIdx.x;
  float v[9]; float s=0;
  for(int i=0;i<9;i++){ v[i]=bf2f(x[row*D+lane+64*i]); s+=v[i]; }
  for(int o=32;o;o>>=1) s+=__shfl_xor(s,o);
  float m = s/D;
  float q=0; for(int i=0;i<9;i++){ float d=v[i]-m; q+=d*d; }
  for(int o=32;o;o>>=1) q+=__shfl_xor(q,o);
  float var = rbf(q/D);
  float mb = rbf(m);
  float den = rbf(sqrtf(rbf(var+1e-5f)));
  for(int i=0;i<9;i++){ float d=rbf(v[i]-mb); xn[row*D+lane+64*i]=f2bf(d/den); }
}
__global__ void gemm_naive(const u16* A, const u16* B, u16* C, int M, int N, int K){
  __shared__ float sa[16][17], sb[16][17];
  int tx=threadIdx.x, ty=threadIdx.y;
  int r=blockIdx.y*16+ty, c=blockIdx.x*16+tx;
  float acc=0;
  for(int k0=0;k0<K;k0+=16){
    sa[ty][tx]=bf2f(A[r*K+k0+tx]);
    sb[ty][tx]=bf2f(B[(k0+ty)*N+c]);
    __syncthreads();
    for(int k=0;k<16;k++) acc+=sa[ty][k]*sb[k][tx];
    __syncthreads();
  }
  C[r*N+c]=f2bf(acc);
}
__global__ void glu_kernel(const u16* g, const u16* u, u16* h, int n){
  int i=blockIdx.x*blockDim.x+threadIdx.x; if(i>=n) return;
  float gg=bf2f(g[i]), uu=bf2f(u[i]);
  float s=rbf(1.f/(1.f+__expf(-gg)));
  h[i]=f2bf(s*uu);
}
__global__ void attn_naive(const u16* qkv, u16* o){
  int idx=blockIdx.x*blockDim.x+threadIdx.x; if(idx>=S*H) return;
  int q=idx/H, h=idx%H;
  float qv[DH]; for(int d=0;d<DH;d++) qv[d]=bf2f(qkv[q*3*D+h*DH+d]);
  int k0=q-W+1; if(k0<0)k0=0;
  float m=-1e30f;
  for(int k=k0;k<=q;k++){ float s=0; for(int d=0;d<DH;d++) s+=qv[d]*bf2f(qkv[k*3*D+D+h*DH+d]); s*=0.23570226f; m=fmaxf(m,s);}
  float l=0; float acc[DH]; for(int d=0;d<DH;d++) acc[d]=0;
  for(int k=k0;k<=q;k++){ float s=0; for(int d=0;d<DH;d++) s+=qv[d]*bf2f(qkv[k*3*D+D+h*DH+d]); s*=0.23570226f; float p=__expf(s-m); l+=p; for(int d=0;d<DH;d++) acc[d]+=p*bf2f(qkv[k*3*D+2*D+h*DH+d]); }
  for(int d=0;d<DH;d++) o[q*D+h*DH+d]=f2bf(acc[d]/l);
}
__global__ void final_kernel(const u16* x, const u16* s, const u16* y, u16* out, int n){
  int i=blockIdx.x*blockDim.x+threadIdx.x; if(i>=n) return;
  float yy=bf2f(y[i]);
  float g=rbf(yy/(1.f+__expf(-yy)));
  float p=rbf(bf2f(s[i])*g);
  out[i]=f2bf(bf2f(x[i])+p);
}

extern "C" void kernel_launch(void* const* d_in, const int* in_sizes, int n_in,
                              void* d_out, int out_size, void* d_ws, size_t ws_size,
                              hipStream_t stream) {
  const u16* x=(const u16*)d_in[0]; const u16* wqkv=(const u16*)d_in[1]; const u16* wo=(const u16*)d_in[2];
  const u16* wg=(const u16*)d_in[4]; const u16* wu=(const u16*)d_in[5]; const u16* wd=(const u16*)d_in[6];
  u16* ws=(u16*)d_ws;
  u16* xn=ws; u16* qkv=xn+S*D; u16* o=qkv+S*3*D; u16* sb=o+S*D; u16* g=sb+S*D; u16* u=g+S*F; u16* h=u+S*F; u16* y=h+S*F;
  ln_kernel<<<S,64,0,stream>>>(x,xn);
  gemm_naive<<<dim3(3*D/16,S/16),dim3(16,16),0,stream>>>(xn,wqkv,qkv,S,3*D,D);
  attn_naive<<<S*H/64,64,0,stream>>>(qkv,o);
  gemm_naive<<<dim3(D/16,S/16),dim3(16,16),0,stream>>>(o,wo,sb,S,D,D);
  gemm_naive<<<dim3(F/16,S/16),dim3(16,16),0,stream>>>(xn,wg,g,S,F,D);
  gemm_naive<<<dim3(F/16,S/16),dim3(16,16),0,stream>>>(xn,wu,u,S,F,D);
  glu_kernel<<<S*F/256,256,0,stream>>>(g,u,h,S*F);
  gemm_naive<<<dim3(D/16,S/16),dim3(16,16),0,stream>>>(h,wd,y,S,D,F);
  final_kernel<<<S*D/256,256,0,stream>>>(x,sb,y,(u16*)d_out,S*D);
}
```

```cpp
#include <hip/hip_runtime.h>
#include <hip/hip_bf16.h>
#include <stdint.h>
#include <utility>

typedef unsigned short u16;
typedef __bf16 bf16x8 __attribute__((ext_vector_type(8)));
typedef __bf16 bf16x2 __attribute__((ext_vector_type(2)));
typedef float f32x16 __attribute__((ext_vector_type(16)));
typedef float f32x4 __attribute__((ext_vector_type(4)));
typedef float f32x2 __attribute__((ext_vector_type(2)));
typedef short s16x4 __attribute__((ext_vector_type(4)));
typedef short s16x8 __attribute__((ext_vector_type(8)));
typedef unsigned u32x4 __attribute__((ext_vector_type(4)));
typedef unsigned u32x2 __attribute__((ext_vector_type(2)));
#define LAS __attribute__((address_space(3)))
#define GAS __attribute__((address_space(1)))
#define DI __device__ __forceinline__

constexpr int S = 2048, D = 576, F = 4608;
constexpr size_t WS_SYNC = 0;
constexpr int SY_GEN = 0;
constexpr int SY_START = 64;
constexpr int SY_XF = 512;
constexpr int SY_QF = SY_XF + 8 * 64;
constexpr int SY_HF = SY_QF + 8 * 64;
constexpr int SY_OF = SY_HF + 64 * 64;
constexpr int SY_PF = SY_OF + 64 * 64;
constexpr int SY_SF = SY_PF + 32 * 64;
constexpr int SY_END = SY_SF + 32 * 64;
static_assert(SY_END * 4 <= 65536, "sync area");
constexpr size_t WS_H = 65536;
constexpr size_t WS_H_BYTES = (size_t)64 * 288 * 1024;
constexpr size_t WS_QKV = WS_H + WS_H_BYTES;
constexpr size_t WS_QKV_BYTES = (size_t)3 * 32 * 2048 * 48;
constexpr size_t WS_O = WS_QKV + WS_QKV_BYTES + 4096;
constexpr size_t WS_O_BYTES = (size_t)128 * 8 * 3 * 1024;
constexpr size_t WS_P = WS_O + WS_O_BYTES;
constexpr size_t WS_P_BYTES = (size_t)8 * 4 * 8 * 8 * 18 * 1024;
constexpr size_t WS_X = WS_P + WS_P_BYTES;
constexpr size_t WS_X_BYTES = (size_t)64 * 36 * 1024;
constexpr size_t WS_USED = WS_X + WS_X_BYTES;

DI float lo2f(unsigned v) { return __uint_as_float(v << 16); }
DI float hi2f(unsigned v) { return __uint_as_float(v & 0xffff0000u); }
DI unsigned pk2(float a, float b) { f32x2 t = {a, b}; return __builtin_bit_cast(unsigned, __builtin_convertvector(t, bf16x2)); }
DI float rbf(float f) { return lo2f(pk2(f, 0.f)); }
DI float dot2(unsigned a, unsigned b, float c) { return __builtin_amdgcn_fdot2_f32_bf16(__builtin_bit_cast(bf16x2, a), __builtin_bit_cast(bf16x2, b), c, false); }
DI float xhalf_max(float v) { auto r = __builtin_amdgcn_permlane32_swap(__float_as_uint(v), __float_as_uint(v), false, false); return fmaxf(__uint_as_float(r[0]), __uint_as_float(r[1])); }
DI float lo_bcast(float v) { auto r = __builtin_amdgcn_permlane32_swap(__float_as_uint(v), __float_as_uint(v), false, false); return __uint_as_float(r[0]); }

DI void glds16(const void* sbase, unsigned voff, unsigned lds_dst) {
    unsigned keep;
    asm volatile("s_mov_b32 %0, m0\n\ts_mov_b32 m0, %3\n\ts_nop 0\n\tglobal_load_lds_dwordx4 %1, %2\n\ts_mov_b32 m0, %0"
                 : "=&s"(keep) : "v"(voff), "s"(sbase), "s"(lds_dst) : "memory");
}
template <int OFF> DI void gload16_sc1(bf16x8& dst, unsigned voff, const char* sbase) {
    asm volatile("global_load_dwordx4 %0, %1, %2 offset:%3 sc1" : "=v"(dst) : "v"(voff), "s"(sbase), "i"(OFF) : "memory");
}
DI void gload4_sc1(unsigned& dst, unsigned voff, const void* sbase) {
    asm volatile("global_load_dword %0, %1, %2 sc1" : "=v"(dst) : "v"(voff), "s"(sbase) : "memory");
}
template <int N> DI void wait_vm_dep(bf16x8& v) { asm volatile("s_waitcnt vmcnt(%1)" : "+v"(v) : "i"(N)); }
template <int N> DI void wait_vm_dep2(bf16x8& v, bf16x8& w) { asm volatile("s_waitcnt vmcnt(%2)" : "+v"(v), "+v"(w) : "i"(N)); }
template <int N> DI void wait_vm_u(unsigned& v) { asm volatile("s_waitcnt vmcnt(%1)" : "+v"(v) : "i"(N)); }
template <int N> DI void wait_vm() { asm volatile("s_waitcnt vmcnt(%0)" ::"i"(N) : "memory"); }
#define WAIT_LGKM0() asm volatile("s_waitcnt lgkmcnt(0)" ::: "memory")
#define BAR() do { asm volatile("s_waitcnt lgkmcnt(0)" ::: "memory"); __builtin_amdgcn_s_barrier(); asm volatile("" ::: "memory"); } while (0)
#define BBAR() do { asm volatile("" ::: "memory"); __builtin_amdgcn_s_barrier(); asm volatile("" ::: "memory"); } while (0)
#define SB() __builtin_amdgcn_sched_barrier(0)
template <int... Is, class Fn> DI void sfor_impl(std::integer_sequence<int, Is...>, Fn&& f) { (f(std::integral_constant<int, Is>{}), ...); }
template <int N, class Fn> DI void sfor(Fn&& f) { sfor_impl(std::make_integer_sequence<int, N>{}, f); }
template <int V> using IC = std::integral_constant<int, V>;
DI unsigned opaque(unsigned v) { asm volatile("" : "+v"(v)); return v; }
DI int fresh_lane() { int l; asm volatile("v_mbcnt_lo_u32_b32 %0, -1, 0\n\tv_mbcnt_hi_u32_b32 %0, -1, %0" : "=v"(l)); return l; }
DI bf16x8 tr_frag_o(unsigned base, int off0, int off1) {
    s16x4 lo = __builtin_amdgcn_ds_read_tr16_b64_v4i16((LAS s16x4*)((LAS char*)(size_t)base + off0));
    s16x4 hi = __builtin_amdgcn_ds_read_tr16_b64_v4i16((LAS s16x4*)((LAS char*)(size_t)base + off1));
    s16x8 f = __builtin_shufflevector(lo, hi, 0, 1, 2, 3, 4, 5, 6, 7);
    return __builtin_bit_cast(bf16x8, f);
}
DI u32x4 lds_rd128(unsigned a) { return *(LAS u32x4*)(size_t)a; }
DI unsigned lds_rd32(unsigned a) { return *(LAS unsigned*)(size_t)a; }
#define MFMA32(a, b, c) __builtin_amdgcn_mfma_f32_32x32x16_bf16((a), (b), (c), 0, 0, 0)
#define MFMA16(a, b, c) __builtin_amdgcn_mfma_f32_16x16x32_bf16((a), (b), (c), 0, 0, 0)

typedef __amdgpu_buffer_rsrc_t rsrc_t;
DI void st16_sc1(rsrc_t r, unsigned off, u32x4 v) { __builtin_amdgcn_raw_buffer_store_b128(v, r, off, 0, 16); }
DI void st4_sc1(rsrc_t r, unsigned off, unsigned v) { __builtin_amdgcn_raw_buffer_store_b32(v, r, off, 0, 16); }
DI void flag_store(unsigned* sy, int idx, unsigned tag) { __hip_atomic_store((GAS unsigned*)sy + idx, tag, __ATOMIC_RELAXED, __HIP_MEMORY_SCOPE_AGENT); }
DI unsigned flag_load(const unsigned* sy, int idx) { return __hip_atomic_load((GAS unsigned*)sy + idx, __ATOMIC_RELAXED, __HIP_MEMORY_SCOPE_AGENT); }
constexpr unsigned kSpinCap = 1u << 20;
DI void wait_flags(const unsigned* sy, int idx, unsigned tag) {
    for (unsigned spins = 0; spins < kSpinCap; ++spins) {
        const unsigned v = flag_load(sy, idx);
        if (__all(v == tag)) break;
        __builtin_amdgcn_s_sleep(1);
    }
    asm volatile("" ::: "memory");
}

#define STAMP(i) do {} while (0)

template <int CTRL, int RMASK> DI float dpp_add(float v) {
    return v + __int_as_float(__builtin_amdgcn_update_dpp(0, __float_as_int(v), CTRL, RMASK, 0xf, true));
}
DI float wave_sum(float v) {
    v = dpp_add<0x111, 0xf>(v); v = dpp_add<0x112, 0xf>(v); v = dpp_add<0x114, 0xf>(v); v = dpp_add<0x118, 0xf>(v);
    v = dpp_add<0x142, 0xa>(v); v = dpp_add<0x143, 0xc>(v);
    return __int_as_float(__builtin_amdgcn_readlane(__float_as_int(v), 63));
}

constexpr int SLOT = 24576;
constexpr int SPARE = SLOT * 6;
constexpr int LDS_BYTES = 163840;
constexpr int KPOS = 4 * SLOT, VPOS = 5 * SLOT;
constexpr int WSL = 27648;
constexpr int WQ0 = 0, WQ1 = WSL, WQ2 = 2 * WSL, WQ3 = 3 * WSL, WQ4 = 4 * WSL, WOP = 5 * WSL;
static_assert(WQ3 <= KPOS && WOP + 21 * 1024 <= LDS_BYTES, "lds");

struct Ctx {
    const char* wq; const char* wgu;
    unsigned voff_q, voff_gu;
    unsigned ldsbase;
    int wave; int P0;
};
DI void issue_q(const Ctx& c, int chunk, int pos) {
#pragma unroll
    for (int i = 0; i < 3; ++i)
        glds16(c.wq + (size_t)(chunk * 192 + i * 8) * 3456, c.voff_q, c.ldsbase + pos * SLOT + (c.wave * 3 + i) * 1024);
}
DI void issue_gu(const Ctx& c, int panel, int chunk, int pos) {
#pragma unroll
    for (int i = 0; i < 3; ++i)
        glds16(c.wgu + (size_t)(chunk * 96 + i * 8) * 9216 + (size_t)(c.P0 + panel) * 128, c.voff_gu, c.ldsbase + pos * SLOT + (c.wave * 3 + i) * 1024);
}

constexpr int PF1 = 3;
template <int NT> constexpr int pn_slot(int idx) { return idx / (6 * NT); }
template <int NT> constexpr int pn_s(int idx) { return (idx / (6 * NT) < 5) ? (idx % (6 * NT)) / NT : (idx % (6 * NT)) % 6; }
template <int NT> constexpr int pn_t(int idx) { return (idx / (6 * NT) < 5) ? (idx % (6 * NT)) % NT : (idx % (6 * NT)) / 6; }

struct Hand {
    unsigned* sy; unsigned tag; rsrc_t rs; int tt, j, c4, cgrp;
    const char* kb; const char* vb; const char* qb;
};

template <int NT>
DI void panel(const Ctx& c, const Hand& hd, const bf16x8 (&xf)[36], int p, int lo, unsigned Lrel, unsigned hoff  , unsigned& qflag) {
    f32x16 acc[NT];
    unsigned cur[NT];
    bf16x8 A[PF1 + 1];
    constexpr int NSTEP = 36 * NT;
    const unsigned hp = hoff + (unsigned)((c.P0 + p) * 4 + lo) * 1024u;
    auto rd = [&](auto IDX) {
        constexpr int idx = IDX, i = pn_slot<NT>(idx), s = pn_s<NT>(idx), t = pn_t<NT>(idx), pos = (3 + i) % 6;
        if constexpr (s == 0) cur[t] = opaque((Lrel ^ (unsigned)((lo + t) << 5)) + pos * SLOT);
        return tr_frag_o(cur[t], s * 2048, s * 2048 + 512);
    };
    auto epi = [&](auto T, auto PART) {
        constexpr int t = T, part = PART;
        if constexpr (part < 4) {
#pragma unroll
            for (int jj = 2 * part; jj < 2 * part + 2; ++jj) {
                const float e = __builtin_amdgcn_exp2f(acc[t][jj] * -1.4426950408889634f);
                acc[t][jj] = __builtin_amdgcn_rcpf(1.f + e) * acc[t][8 + jj];
            }
        } else {
            u32x4 w = {pk2(acc[t][0], acc[t][1]), pk2(acc[t][2], acc[t][3]), pk2(acc[t][4], acc[t][5]), pk2(acc[t][6], acc[t][7])};
            st16_sc1(hd.rs, hp + t * 1024, w);
        }
    };
    sfor<NSTEP>([&](auto IDX) {
        constexpr int idx = IDX, i = pn_slot<NT>(idx), s = pn_s<NT>(idx), t = pn_t<NT>(idx);
        if constexpr (idx % (6 * NT) == 0) {
            if (p < 2) wait_vm<9>();
            else { if constexpr (i <= 1) wait_vm<9>(); else if constexpr (i <= 4) wait_vm<6>(); }
            if constexpr (i == 5) {
                if (p == 1 && c.wave == 0) {
                    asm volatile("" : "+v"(qflag));
                    if (!__all(qflag == hd.tag)) { const int ln = fresh_lane(); wait_flags(hd.sy, SY_QF + ((ln >= 32 && hd.tt > 0) ? hd.tt - 1 : hd.tt) * 64 + (ln & 31), hd.tag); }
                    asm volatile("buffer_inv sc1" ::: "memory");
                }
            }
            BBAR();
            if constexpr (i == 3) { if (p == 0 && c.wave == 0 && fresh_lane() == 0) flag_store(hd.sy, SY_QF + hd.tt * 64 + hd.cgrp, hd.tag); }
            const int pn = p + (5 + i) / 6;
            if (pn < 3) issue_gu(c, pn, (5 + i) % 6, (2 + i) % 6);
            if constexpr (i == 1) { if (p == 1 && c.wave == 0) { const int ln = fresh_lane(); gload4_sc1(qflag, (unsigned)(SY_QF + ((ln >= 32 && hd.tt > 0) ? hd.tt - 1 : hd.tt) * 64 + (ln & 31)) * 4u, hd.sy); } }
            if constexpr (i == 2 || i == 3) {
                if (p == 2) {
                    const unsigned ln16 = (unsigned)fresh_lane() * 16u;
#pragma unroll
                    for (int k = 0; k < 3; ++k) {
                        const int m = c.wave * 3 + k;
                        int off = (hd.tt * 256 - 256) * 48 + m * 1024;
                        if (off < 0) off = m * 1024;
                        glds16((i == 2 ? hd.kb : hd.vb) + off, ln16, c.ldsbase + (i == 2 ? KPOS : VPOS) + m * 1024);
                    }
                }
            }
            if constexpr (i == 4) {
                if (p == 2) {
                    const unsigned ln16 = (unsigned)fresh_lane() * 16u;
#pragma unroll
                    for (int k = 0; k < 2; ++k) {
                        int m = c.wave * 2 + k; if (m > 11) m = 11;
                        glds16(hd.qb + m * 1024, ln16, c.ldsbase + SPARE + m * 1024);
                    }
                }
            }
            if constexpr (idx == 0) sfor<PF1>([&](auto J) { A[J] = rd(J); });
        }
        if constexpr (idx + PF1 < NSTEP) A[(idx + PF1) % (PF1 + 1)] = rd(IC<idx + PF1>{});
        SB();
        if constexpr (i == 0 && s == 0) {
            f32x16 z;
#pragma unroll
            for (int r = 0; r < 16; ++r) z[r] = 0.f;
            acc[t] = MFMA32(A[idx % (PF1 + 1)], xf[6 * i + s], z);
        } else acc[t] = MFMA32(A[idx % (PF1 + 1)], xf[6 * i + s], acc[t]);
        SB();
        if constexpr (i == 5 && t > 0 && s >= 1) { epi(IC<t - 1>{}, IC<s - 1>{}); SB(); }
    });
    sfor<5>([&](auto PART) { epi(IC<NT - 1>{}, PART); });
}

constexpr int XLA = 16;
constexpr int xwait(int s) { return ((s + XLA > 35 ? 35 : s + XLA) - s) + ((s >= 12 && s < 12 + XLA) ? 3 : 0) + ((s >= 24 && s < 24 + XLA) ? 3 : 0); }
constexpr int HLA = 8;
constexpr int hwait(int s) {
    int last = s + HLA > 17 ? 17 : s + HLA;
    int n = 2 * (last - s);
    if (s < HLA) n += 11;
    if (s >= 3 && s < 3 + HLA) n += 4;
    if (s > 9) n += 6;
    return n;
}
constexpr int g2_after_wo(int ks) { int last = ks - 1 + HLA > 17 ? 17 : ks - 1 + HLA; return 2 * (last - HLA + 1) + 4; }

__global__ __launch_bounds__(512, 2) void mega_kernel(const u16* __restrict__ x, const u16* __restrict__ wqkv, const u16* __restrict__ wo,
                                                      const u16* __restrict__ wg, const u16* __restrict__ wu, const u16* __restrict__ wd,
                                                      char* __restrict__ ws, u16* __restrict__ out) {
    extern __shared__ __attribute__((aligned(16))) char smem[];
    const int tid = threadIdx.x, lane = tid & 63;
    const int wave = __builtin_amdgcn_readfirstlane(tid >> 6);
    const int bid = blockIdx.x, xj = bid & 7, loc = bid >> 3;
    const int c4 = loc & 3, tt = loc >> 2, cgrp = xj * 4 + c4;
    const unsigned ldsbase = (unsigned)(size_t)(LAS char*)smem;
    unsigned* sy = (unsigned*)(ws + WS_SYNC);
    const rsrc_t rs = __builtin_amdgcn_make_buffer_rsrc(ws, 0, (unsigned)WS_USED, 0x00020000);

    STAMP(0);
    const int xrow = bid * 8 + wave;
    u32x4 xv0, xv1 = {0u, 0u, 0u, 0u};
    {
        const char* xr = (const char*)x + (size_t)xrow * 1152;
        xv0 = *(const u32x4*)(xr + lane * 16);
        xv1 = *(const u32x4*)(xr + 1024 + (lane & 7) * 16);
    }
    const unsigned gen = flag_load(sy, SY_GEN);

    Ctx c;
    const int qa0 = (108 * cgrp) & ~15;
    const int qdelta = (108 * cgrp - qa0) >> 1;
    c.wq = (const char*)wqkv + qa0; c.wgu = (const char*)((wave >> 2) ? wu : wg);
    c.ldsbase = ldsbase; c.wave = wave; c.P0 = (9 * cgrp) >> 2;
    {
        const int lrow = lane >> 3, c16 = lane & 7, bsel = wave >> 2;
        const int swgu = ((lrow >> 1) & 1) | (bsel << 1);
        const int chgu = (((c16 >> 1) ^ swgu) << 1) | (c16 & 1);
        c.voff_gu = (unsigned)((24 * (wave & 3) + lrow) * 9216 + chgu * 16);
        const int swq = ((lrow >> 1) & 1) << 1;
        int chq = (((c16 >> 1) ^ swq) << 1) | (c16 & 1);
        if (cgrp == 31 && chq == 7) chq = 6;
        c.voff_q = (unsigned)((24 * wave + lrow) * 3456 + chq * 16);
    }
    const unsigned tag = (unsigned)__builtin_amdgcn_readfirstlane((int)(gen * 0x9E3779B1u + 0x7F4A7C15u));
    {
        float s1 = 0.f;
        if (lane >= 8) xv1 = (u32x4){0u, 0u, 0u, 0u};
#pragma unroll
        for (int k = 0; k < 4; ++k) { s1 = dot2(xv0[k], 0x3f803f80u, s1); s1 = dot2(xv1[k], 0x3f803f80u, s1); }
        const float meanf = wave_sum(s1) * (1.f / 576.f);
        float s2 = 0.f;
#pragma unroll
        for (int k = 0; k < 4; ++k) {
            float a = lo2f(xv0[k]) - meanf, b = hi2f(xv0[k]) - meanf; s2 += a * a + b * b;
            if (lane < 8) { a = lo2f(xv1[k]) - meanf; b = hi2f(xv1[k]) - meanf; s2 += a * a + b * b; }
        }
        const float var = rbf(wave_sum(s2) * (1.f / 576.f));
        const float mean = rbf(meanf);
        const float den = rbf(sqrtf(rbf(var + 1e-5f)));
        u32x4 o0, o1;
#pragma unroll
        for (int k = 0; k < 4; ++k) {
            o0[k] = pk2(rbf(lo2f(xv0[k]) - mean) / den, rbf(hi2f(xv0[k]) - mean) / den);
            o1[k] = pk2(rbf(lo2f(xv1[k]) - mean) / den, rbf(hi2f(xv1[k]) - mean) / den);
        }
        *(LAS u32x4*)(size_t)(ldsbase + SPARE + (lane * 8 + wave) * 16) = o0;
        if (lane < 8) *(LAS u32x4*)(size_t)(ldsbase + SPARE + ((64 + lane) * 8 + wave) * 16) = o1;
        BAR();
        const unsigned xd = (unsigned)WS_X + (unsigned)(bid >> 2) * 36u * 1024u;
        const int t0 = (bid & 3) * 8;
        {
            const int i = tid, cc = i >> 3, tk = i & 7;
            st16_sc1(rs, xd + ((cc >> 1) * 64 + (cc & 1) * 32 + t0 + tk) * 16, lds_rd128(ldsbase + SPARE + i * 16));
        }
        if (wave == 0) {
            const int i = 512 + lane, cc = i >> 3, tk = i & 7;
            st16_sc1(rs, xd + ((cc >> 1) * 64 + (cc & 1) * 32 + t0 + tk) * 16, lds_rd128(ldsbase + SPARE + i * 16));
        }
        issue_q(c, 0, 0); issue_q(c, 1, 1); issue_q(c, 2, 2);
        wait_vm<9>();
        BBAR();
        if (tid == 0) { flag_store(sy, SY_XF + (bid >> 5) * 64 + (bid & 31), tag); flag_store(sy, SY_START + bid, tag); }
        issue_gu(c, 0, 0, 3); issue_gu(c, 0, 1, 4); issue_gu(c, 0, 2, 5);
    }
    STAMP(1);
    Hand hd;
    hd.sy = sy; hd.tag = tag; hd.rs = rs; hd.tt = tt; hd.j = xj; hd.c4 = c4; hd.cgrp = cgrp;
    hd.kb = ws + WS_QKV + ((size_t)(32 + cgrp) * 2048) * 48; hd.vb = ws + WS_QKV + ((size_t)(64 + cgrp) * 2048) * 48;
    hd.qb = ws + WS_QKV + ((size_t)cgrp * 2048 + tt * 256) * 48;

    if (wave == 0) wait_flags(sy, SY_XF + tt * 64 + (lane & 31), tag);
    wait_vm<0>();
    BBAR();
    STAMP(2);
    const int tile32 = tt * 8 + wave;
    bf16x8 xf[36];
    const char* xs = ws + WS_X + (size_t)tile32 * 36 * 1024;
    const unsigned l16 = lane * 16;
    sfor<XLA>([&](auto SI) { constexpr int s = SI; gload16_sc1<(s & 3) * 1024>(xf[s], l16, xs + (s >> 2) * 4096); });

    const int hh = lane >> 5, blk = (lane >> 4) & 1, q = (lane & 15) >> 2, pp = lane & 3;
    {
        const unsigned Lq = ldsbase + (unsigned)((8 * hh + q) * 128 + 8 * pp + 32 * (blk ^ (((q >> 1) & 1) << 1)));
        f32x16 acc[2];
        unsigned cur[2];
        bf16x8 A[PF1 + 1];
        auto rd = [&](auto IDX) {
            constexpr int idx = IDX, i = idx / 24, s = (idx % 24) / 2, t = idx % 2;
            if constexpr (s == 0) cur[t] = opaque((Lq ^ (unsigned)(t << 6)) + i * SLOT);
            return tr_frag_o(cur[t], s * 2048, s * 2048 + 512);
        };
        sfor<72>([&](auto IDX) {
            constexpr int idx = IDX, i = idx / 24, s = (idx % 24) / 2, t = idx % 2, ks = 12 * i + s;
            if constexpr (idx % 24 == 0) {
                if constexpr (i > 0) { BBAR(); issue_gu(c, 0, 2 + i, (5 + i) % 6); }
                if constexpr (idx == 0) sfor<PF1>([&](auto J) { A[J] = rd(J); });
            }
            if constexpr (idx + PF1 < 72) A[(idx + PF1) % (PF1 + 1)] = rd(IC<idx + PF1>{});
            if constexpr (t == 0) {
                if constexpr (ks + XLA < 36) gload16_sc1<((ks + XLA) & 3) * 1024>(xf[ks + XLA], l16, xs + ((ks + XLA) >> 2) * 4096);
                wait_vm_dep<xwait(ks)>(xf[ks]);
            }
            SB();
            if constexpr (idx < 2) {
                f32x16 z;
#pragma unroll
                for (int r = 0; r < 16; ++r) z[r] = 0.f;
                acc[t] = MFMA32(A[idx % (PF1 + 1)], xf[ks], z);
            } else acc[t] = MFMA32(A[idx % (PF1 + 1)], xf[ks], acc[t]);
            SB();
        });
        STAMP(3);
        BAR();
        const unsigned st01 = ldsbase + 2 * SLOT + wave * 3072, st2 = ldsbase + SPARE + wave * 2048;
        const int t32 = lane & 31;
#pragma unroll
        for (int t = 0; t < 2; ++t) {
#pragma unroll
            for (int r = 0; r < 16; r += 2) {
                const int n = t * 32 + (r & 3) + 8 * (r >> 2) + 4 * hh - qdelta;
                const int sl = (n * 3641) >> 16, d = n - sl * 18;
                unsigned ad = (sl < 2 ? st01 + sl * 1536 : st2) + t32 * 48 + d * 2;
                if (n < 0 || n >= 54) ad = st2 + 1536 + lane * 4;
                *(LAS unsigned*)(size_t)ad = pk2(acc[t][r], acc[t][r + 1]);
            }
        }
#pragma unroll
        for (int sl = 0; sl < 3; ++sl) {
            const unsigned rb = (sl < 2 ? st01 + sl * 1536 : st2) + t32 * 48;
            if (hh == 0) *(LAS unsigned*)(size_t)(rb + 36) = (cgrp * 3 + sl >= 64) ? 0x00003F80u : 0u;
            else *(LAS u32x2*)(size_t)(rb + 40) = (u32x2){0u, 0u};
        }
        WAIT_LGKM0();
        const unsigned qdst = (unsigned)WS_QKV + (unsigned)((cgrp * 3) * 2048 + tile32 * 32) * 48u;
#pragma unroll
        for (int k = 0; k < 5; ++k) {
            const int i = lane + 64 * k;
            if (k < 4 || lane < 32) {
                const int sl = i / 96, rem = i - sl * 96;
                const u32x4 w = lds_rd128((sl < 2 ? st01 + sl * 1536 : st2) + rem * 16);
                st16_sc1(rs, qdst + (unsigned)sl * 2048u * 48u + rem * 16, w);
            }
        }
        WAIT_LGKM0();
    }
    STAMP(4);
    {
        const unsigned Lgu = ldsbase + (unsigned)(blk * 12288 + (8 * hh + q) * 128 + 8 * pp + 32 * (((q >> 1) & 1) | (blk << 1)));
        const unsigned hoff = (unsigned)WS_H + ((unsigned)tile32 * 288u * 64u + lane) * 16u;
        unsigned qflag = 0;
#pragma unroll 1
        for (int p = 0; p < 3; ++p) {
            const int nt = (p == 0) ? 4 - c4 : (p == 1 ? 4 : 1 + c4);
            const int lo = (p == 0) ? c4 : 0;
            switch (nt) {
                case 1: panel<1>(c, hd, xf, p, lo, Lgu, hoff, qflag); break;
                case 2: panel<2>(c, hd, xf, p, lo, Lgu, hoff, qflag); break;
                case 3: panel<3>(c, hd, xf, p, lo, Lgu, hoff, qflag); break;
                default: panel<4>(c, hd, xf, p, lo, Lgu, hoff, qflag); break;
            }
            STAMP(5 + p);
        }
    }

    {
    const int lane = fresh_lane();
    const int hh = lane >> 5, blk = (lane >> 4) & 1, q = (lane & 15) >> 2, pp = lane & 3;
    const unsigned l16 = lane * 16;
    const bool tid0 = (wave == 0) && (lane == 0);
    const char* wdb = (const char*)wd + (size_t)(xj * 576) * 1152 + c4 * 288;
    unsigned vo[4];
#pragma unroll
    for (int i = 0; i < 4; ++i) {
        int m = wave + 8 * i; if (m >= 27) m -= 27;
        const int n = m * 64 + lane, row = n / 18, cc = n - row * 18;
        vo[i] = (unsigned)(row * 1152 + cc * 16);
    }
    auto issue_wd = [&](int slot, int dst) {
#pragma unroll
        for (int i = 0; i < 4; ++i) {
            int m = wave + 8 * i; if (m >= 27) m -= 27;
            glds16(wdb + (size_t)slot * 96 * 1152, vo[i], ldsbase + dst + m * 1024);
        }
    };
    unsigned hflag = 0;
    {
        if (c4 == 0) wait_vm<1>(); else if (c4 == 1) wait_vm<2>(); else if (c4 == 2) wait_vm<3>(); else wait_vm<4>();
        BBAR();
        issue_wd(0, WQ0); issue_wd(1, WQ1); issue_wd(2, WQ2);
        STAMP(8);
        const int t = lane & 31;
        const int q0 = tt * 256 + wave * 32;
        bf16x8 Qf0, Qf1;
        {
            const unsigned qa = ldsbase + SPARE + (wave * 32 + t) * 48;
            Qf0 = __builtin_bit_cast(bf16x8, lds_rd128(qa + 16 * hh));
            const unsigned qv1 = lds_rd32(qa + 32);
            u32x4 q1 = {hh == 0 ? qv1 : 0u, 0u, 0u, 0u};
            Qf1 = __builtin_bit_cast(bf16x8, q1);
        }
        const unsigned kaddr = ldsbase + KPOS + (wave * 32 + t) * 48;
        const unsigned vaddr = ldsbase + VPOS + (wave * 32 + 4 * hh + q) * 48 + 32 * blk + 8 * pp;
        constexpr float CS = 0.23570226039551584f * 1.4426950408889634f;
        const float NEG = -__builtin_inff();
        f32x16 o;
        auto attend = [&](auto FULLC) {
            constexpr bool FULL = FULLC;
            const int ktmin = FULL ? 0 : 8 - wave;
            f32x16 sc[9];
#pragma unroll
            for (int kt = 0; kt < 9; ++kt) {
                if (FULL || kt >= ktmin) {
                    u32x4 k0 = lds_rd128(kaddr + kt * 1536 + 16 * hh);
                    u32x4 k1 = {lds_rd32(kaddr + kt * 1536 + 32), 0u, 0u, 0u};
                    f32x16 z;
#pragma unroll
                    for (int r = 0; r < 16; ++r) z[r] = 0.f;
                    z = MFMA32(__builtin_bit_cast(bf16x8, k0), Qf0, z);
                    sc[kt] = MFMA32(__builtin_bit_cast(bf16x8, k1), Qf1, z);
                } else {
#pragma unroll
                    for (int r = 0; r < 16; ++r) sc[kt][r] = NEG;
                }
            }
#pragma unroll
            for (int r = 0; r < 16; ++r) {
                const int kk = (r & 3) + 8 * (r >> 2) + 4 * hh;
                if (!(kk > t)) sc[0][r] = NEG;
                if (!(kk <= t)) sc[8][r] = NEG;
            }
            float m = NEG;
#pragma unroll
            for (int kt = 0; kt < 9; ++kt)
#pragma unroll
                for (int r = 0; r < 16; ++r) m = fmaxf(m, sc[kt][r]);
            m = xhalf_max(m);
            wait_vm<12>();
            BBAR();
            if (tid0) flag_store(sy, SY_HF + (xj * 8 + tt) * 64 + c4, tag);
            if (wave == 0) gload4_sc1(hflag, (unsigned)(SY_HF + (xj * 8 + tt) * 64 + (lane & 3)) * 4u, sy);
            const float mc = -m * CS;
#pragma unroll
            for (int r = 0; r < 16; ++r) o[r] = 0.f;
#pragma unroll
            for (int kt = 0; kt < 9; ++kt) {
                if (FULL || kt >= ktmin) {
                    unsigned pk[8];
#pragma unroll
                    for (int r = 0; r < 16; r += 2)
                        pk[r >> 1] = pk2(__builtin_amdgcn_exp2f(fmaf(sc[kt][r], CS, mc)), __builtin_amdgcn_exp2f(fmaf(sc[kt][r + 1], CS, mc)));
#pragma unroll
                    for (int s = 0; s < 2; ++s) {
                        u32x4 pb = {pk[4 * s], pk[4 * s + 1], pk[4 * s + 2], pk[4 * s + 3]};
                        bf16x8 A = tr_frag_o(vaddr, (kt * 32 + 16 * s) * 48, (kt * 32 + 16 * s + 8) * 48);
                        o = MFMA32(A, __builtin_bit_cast(bf16x8, pb), o);
                    }
                }
            }
        };
        if (tt > 0) attend(std::true_type{}); else attend(std::false_type{});
        const float inv = 1.f / lo_bcast(o[10]);
        const int tile16 = (q0 >> 4) + ((lane >> 4) & 1), t16 = lane & 15;
        const unsigned ob = (unsigned)WS_O + (unsigned)((tile16 * 8 + xj) * 3) * 1024u + t16 * 16;
        u32x4 w = {pk2(o[0] * inv, o[1] * inv), pk2(o[2] * inv, o[3] * inv), pk2(o[4] * inv, o[5] * inv), pk2(o[6] * inv, o[7] * inv)};
        st16_sc1(rs, ob + (c4 >> 1) * 1024 + (2 * (c4 & 1) + hh) * 256, w);
        st4_sc1(rs, hh == 0 ? ob + 2 * 1024 + c4 * 4 : 0xFFFFFFF0u, pk2(o[8] * inv, o[9] * inv));
        if (wave == 0) {
            wait_vm_u<2>(hflag);
            if (!__all(hflag == tag)) wait_flags(sy, SY_HF + (xj * 8 + tt) * 64 + (lane & 3), tag);
        }
    }
    STAMP(9);
    {
        wait_vm<2>();
        BAR();
        const int q4 = lane >> 4, t16 = lane & 15, qq = (lane & 15) >> 2;
        const int tile32 = tt * 8 + wave;
        const char* hsrc = ws + WS_H + ((size_t)tile32 * 288 + xj * 36) * 1024;
        const unsigned hvo = (unsigned)(((q4 >> 1) * 64 + (q4 & 1) * 32 + t16) * 16);
        bf16x8 hf[18][2];
        auto hload = [&](auto SI) { constexpr int s = SI;
            gload16_sc1<(s & 1) * 2048>(hf[s][0], hvo, hsrc + (s >> 1) * 4096);
            gload16_sc1<(s & 1) * 2048 + 256>(hf[s][1], hvo, hsrc + (s >> 1) * 4096); };
        sfor<HLA>([&](auto SI) { hload(SI); });
        issue_wd(3, WQ3); issue_wd(4, WQ4);
        {
#pragma unroll
            for (int i = 0; i < 3; ++i) {
                int m = wave * 3 + i; if (m > 20) m = 20;
                int n = m * 64 + lane; if (n > 1295) n = 1295;
                const int row = n / 18, cc = n - row * 18;
                glds16((const char*)wo + (size_t)(xj * 72) * 1152 + c4 * 288, (unsigned)(row * 1152 + cc * 16), ldsbase + WOP + m * 1024);
            }
        }
        f32x4 acc[2][9];
        const unsigned arel = ldsbase + (unsigned)((16 * (q4 >> 1) + 4 * (q4 & 1) + qq) * 288 + 8 * pp);
        constexpr int PF = 4, NST = 6 * 27;
        bf16x8 A[PF + 1];
        unsigned cur = 0;
        unsigned oflag = 0, sflag = 0;
        bf16x8 of[3][2];
        const unsigned pdst = (unsigned)WS_P + ((unsigned)(((tt * 4 + c4) * 8 + wave) * 8 + xj) * 18u * 64u + lane) * 16u;
        auto rd = [&](auto IDX) {
            constexpr int idx = IDX, sl = idx / 27, u = (idx % 27) / 9, dt = idx % 9;
            constexpr int place = sl == 0 ? WQ0 : sl == 1 ? WQ1 : sl == 2 ? WQ2 : sl == 3 ? WQ3 : sl == 4 ? WQ4 : WQ0;
            if constexpr (idx % 27 == 0) cur = opaque(arel + place);
            return tr_frag_o(cur, u * 32 * 288 + dt * 32, u * 32 * 288 + dt * 32 + 8 * 288);
        };
        sfor<NST>([&](auto IDX) {
            constexpr int idx = IDX, sl = idx / 27, u = (idx % 27) / 9, dt = idx % 9, ks = 3 * sl + u;
            if constexpr (idx % 27 == 0) {
                STAMP(10 + sl);
                if constexpr (sl == 1) {
                    BBAR();
                    if (tid0) flag_store(sy, SY_OF + (xj * 8 + tt) * 64 + c4, tag);
                    issue_wd(5, WQ0);
                }
                if constexpr (sl == 2) {
                    wait_vm<g2_after_wo(6)>();
                    BBAR();
                    if (wave == 0) gload4_sc1(oflag, (unsigned)(SY_OF + (xj * 8 + tt) * 64 + (lane & 3)) * 4u, sy);
                }
                if constexpr (sl == 3) {
                    if (wave == 0) {
                        wait_vm_u<2 * 3>(oflag);
                        if (!__all(oflag == tag)) wait_flags(sy, SY_OF + (xj * 8 + tt) * 64 + (lane & 3), tag);
                    }
                    BBAR();
                }
                if constexpr (sl == 4) BBAR();
                if constexpr (sl == 5) {
                    wait_vm<0>();
                    BBAR();
                    if (tid0) flag_store(sy, SY_SF + (tt * 4 + c4) * 64 + xj, tag);
                    if (wave == 0) gload4_sc1(sflag, (unsigned)(SY_SF + (tt * 4 + c4) * 64 + (lane & 7)) * 4u, sy);
                }
                if constexpr (idx == 0) sfor<PF>([&](auto J) { A[J] = rd(J); });
            }
            if constexpr (idx + PF < NST) A[(idx + PF) % (PF + 1)] = rd(IC<idx + PF>{});
            if constexpr (dt == 0) {
                if constexpr (ks + HLA < 18) hload(IC<ks + HLA>{});
                if constexpr (ks < 15) wait_vm_dep2<hwait(ks)>(hf[ks][0], hf[ks][1]);
                else asm volatile("" : "+v"(hf[ks][0]), "+v"(hf[ks][1]));
                if constexpr (ks == 9) {
                    const char* osrc = ws + WS_O + ((size_t)(tile32 * 2) * 8 + xj) * 3 * 1024;
                    gload16_sc1<0>(of[0][0], l16, osrc); gload16_sc1<1024>(of[1][0], l16, osrc); gload16_sc1<2048>(of[2][0], l16, osrc);
                    gload16_sc1<0>(of[0][1], l16, osrc + 8 * 3 * 1024); gload16_sc1<1024>(of[1][1], l16, osrc + 8 * 3 * 1024); gload16_sc1<2048>(of[2][1], l16, osrc + 8 * 3 * 1024);
                }
            }
            SB();
            if constexpr (ks == 0) {
                acc[0][dt] = MFMA16(A[idx % (PF + 1)], hf[ks][0], ((f32x4){0.f, 0.f, 0.f, 0.f}));
                acc[1][dt] = MFMA16(A[idx % (PF + 1)], hf[ks][1], ((f32x4){0.f, 0.f, 0.f, 0.f}));
            } else {
                acc[0][dt] = MFMA16(A[idx % (PF + 1)], hf[ks][0], acc[0][dt]);
                acc[1][dt] = MFMA16(A[idx % (PF + 1)], hf[ks][1], acc[1][dt]);
            }
            SB();
            if constexpr (ks == 13) {
                if constexpr (dt == 0) {
                    wait_vm<0>();
                    asm volatile("" : "+v"(of[0][0]), "+v"(of[0][1]), "+v"(of[1][0]), "+v"(of[1][1]), "+v"(of[2][0]), "+v"(of[2][1]));
                    if (q4 != 0) {
                        const u32x4 zz = {0u, 0u, 0u, 0u};
                        of[2][0] = __builtin_bit_cast(bf16x8, zz); of[2][1] = __builtin_bit_cast(bf16x8, zz);
                    }
                }
                const unsigned wb = ldsbase + WOP + 8 * pp;
                const unsigned r01 = wb + (unsigned)((18 * (q4 >> 1) + 4 * (q4 & 1) + qq) * 288);
                const unsigned r2 = wb + (unsigned)((18 * (qq >> 1) + 16 + (qq & 1)) * 288);
                f32x4 s0 = {0.f, 0.f, 0.f, 0.f}, s1 = {0.f, 0.f, 0.f, 0.f};
                bf16x8 A0 = tr_frag_o(r01, dt * 32, dt * 32 + 8 * 288);
                bf16x8 A1 = tr_frag_o(r01, 36 * 288 + dt * 32, 36 * 288 + dt * 32 + 8 * 288);
                bf16x8 A2 = tr_frag_o(r2, dt * 32, dt * 32 + 36 * 288);
                s0 = MFMA16(A0, of[0][0], s0); s1 = MFMA16(A0, of[0][1], s1);
                s0 = MFMA16(A1, of[1][0], s0); s1 = MFMA16(A1, of[1][1], s1);
                s0 = MFMA16(A2, of[2][0], s0); s1 = MFMA16(A2, of[2][1], s1);
                u32x4 w = {pk2(s0[0], s0[1]), pk2(s0[2], s0[3]), pk2(s1[0], s1[1]), pk2(s1[2], s1[3])};
                st16_sc1(rs, pdst + (9 + dt) * 1024, w);
                SB();
            }
        });
        STAMP(16);
#pragma unroll
        for (int dt = 0; dt < 9; ++dt) {
            u32x4 w = {pk2(acc[0][dt][0], acc[0][dt][1]), pk2(acc[0][dt][2], acc[0][dt][3]), pk2(acc[1][dt][0], acc[1][dt][1]), pk2(acc[1][dt][2], acc[1][dt][3])};
            st16_sc1(rs, pdst + dt * 1024, w);
        }
        if (wave == 0) {
            wait_vm_u<9>(sflag);
            if (!__all(sflag == tag)) wait_flags(sy, SY_SF + (tt * 4 + c4) * 64 + (lane & 7), tag);
        }
        wait_vm<0>();
        BBAR();
        if (tid0) flag_store(sy, SY_PF + (tt * 4 + c4) * 64 + xj, tag);
    }
    }
    STAMP(17);
    {
        const int lane = fresh_lane();
        const int q4 = lane >> 4, t16 = lane & 15;
        const int tok0 = tt * 256 + xj * 32;
        const unsigned psrc = (unsigned)WS_P + ((unsigned)(((tt * 4 + c4) * 8 + xj) * 8) * 18u * 64u + lane) * 16u;
        auto fin = [&](auto NDC) {
            constexpr int ND = NDC;
            u32x2 xv[ND][2];
            u32x4 vs[ND][8], vy[ND][8];
#pragma unroll
            for (int k = 0; k < ND; ++k) {
                const int dt = (k == 0) ? wave : 8;
#pragma unroll
                for (int sj = 0; sj < 8; ++sj) vs[k][sj] = __builtin_amdgcn_raw_buffer_load_b128(rs, psrc + (sj * 18 + 9 + dt) * 1024, 0, 16);
#pragma unroll
                for (int a = 0; a < 2; ++a)
                    xv[k][a] = *(const u32x2*)((const char*)x + (size_t)(tok0 + a * 16 + t16) * 1152 + (c4 * 144 + dt * 16 + 4 * q4) * 2);
            }
            if (ND == 2) wait_flags(sy, SY_PF + (tt * 4 + c4) * 64 + (lane & 7), tag);
            BBAR();
            STAMP(18);
#pragma unroll
            for (int k = 0; k < ND; ++k) {
                const int dt = (k == 0) ? wave : 8;
#pragma unroll
                for (int sj = 0; sj < 8; ++sj) vy[k][sj] = __builtin_amdgcn_raw_buffer_load_b128(rs, psrc + (sj * 18 + dt) * 1024, 0, 16);
            }
#pragma unroll
            for (int k = 0; k < ND; ++k) {
                const int dt = (k == 0) ? wave : 8;
                float y[8], sv[8];
#pragma unroll
                for (int e = 0; e < 8; ++e) { y[e] = 0.f; sv[e] = 0.f; }
#pragma unroll
                for (int sj = 0; sj < 8; ++sj)
#pragma unroll
                    for (int e = 0; e < 4; ++e) { sv[2 * e] += lo2f(vs[k][sj][e]); sv[2 * e + 1] += hi2f(vs[k][sj][e]); }
#pragma unroll
                for (int sj = 0; sj < 8; ++sj)
#pragma unroll
                    for (int e = 0; e < 4; ++e) { y[2 * e] += lo2f(vy[k][sj][e]); y[2 * e + 1] += hi2f(vy[k][sj][e]); }
#pragma unroll
                for (int a = 0; a < 2; ++a) {
                    float ov[4];
#pragma unroll
                    for (int r = 0; r < 4; ++r) {
                        const float yy = rbf(y[4 * a + r]);
                        const float g = rbf(yy * __builtin_amdgcn_rcpf(1.f + __builtin_amdgcn_exp2f(-yy * 1.4426950408889634f)));
                        const float pr = rbf(rbf(sv[4 * a + r]) * g);
                        const unsigned xw = xv[k][a][r >> 1];
                        ov[r] = ((r & 1) ? hi2f(xw) : lo2f(xw)) + pr;
                    }
                    u32x2 w = {pk2(ov[0], ov[1]), pk2(ov[2], ov[3])};
                    *(u32x2*)((char*)out + (size_t)(tok0 + a * 16 + t16) * 1152 + (c4 * 144 + dt * 16 + 4 * q4) * 2) = w;
                }
            }
        };
        if (wave == 0) fin(IC<2>{}); else fin(IC<1>{});
        if (bid == 0 && wave == 7) {
            for (unsigned spins = 0; spins < kSpinCap; ++spins) {
                bool ok = true;
#pragma unroll
                for (int k = 0; k < 4; ++k) ok &= flag_load(sy, SY_START + lane + 64 * k) == tag;
                if (__all(ok)) break;
                __builtin_amdgcn_s_sleep(1);
            }
            if (lane == 0) flag_store(sy, SY_GEN, tag);
        }
    }
    STAMP(19);
}

extern "C" void kernel_launch(void* const* d_in, const int* in_sizes, int n_in, void* d_out, int out_size, void* d_ws, size_t ws_size,
                              hipStream_t stream) {
    const u16* x = (const u16*)d_in[0];
    const u16* wqkv = (const u16*)d_in[1];
    const u16* wo = (const u16*)d_in[2];
    const u16* wg = (const u16*)d_in[4];
    const u16* wu = (const u16*)d_in[5];
    const u16* wd = (const u16*)d_in[6];
    (void)hipFuncSetAttribute((const void*)mega_kernel, hipFuncAttributeMaxDynamicSharedMemorySize, LDS_BYTES);
    mega_kernel<<<256, 512, LDS_BYTES, stream>>>(x, wqkv, wo, wg, wu, wd, (char*)d_ws, (u16*)d_out);
}
```

```cpp
#include <hip/hip_runtime.h>
#include <hip/hip_bf16.h>
#include <stdint.h>
#include <utility>

typedef unsigned short u16;
typedef __bf16 bf16x8 __attribute__((ext_vector_type(8)));
typedef __bf16 bf16x2 __attribute__((ext_vector_type(2)));
typedef float f32x16 __attribute__((ext_vector_type(16)));
typedef float f32x4 __attribute__((ext_vector_type(4)));
typedef float f32x2 __attribute__((ext_vector_type(2)));
typedef short s16x4 __attribute__((ext_vector_type(4)));
typedef short s16x8 __attribute__((ext_vector_type(8)));
typedef unsigned u32x4 __attribute__((ext_vector_type(4)));
typedef unsigned u32x2 __attribute__((ext_vector_type(2)));
#define LAS __attribute__((address_space(3)))
#define GAS __attribute__((address_space(1)))
#define DI __device__ __forceinline__

constexpr int S = 2048, D = 576, F = 4608;
constexpr size_t WS_SYNC = 0;
constexpr int SY_GEN = 0;
constexpr int SY_START = 64;
constexpr int SY_XF = 512;
constexpr int SY_QF = SY_XF + 8 * 64;
constexpr int SY_HF = SY_QF + 8 * 64;
constexpr int SY_OF = SY_HF + 64 * 64;
constexpr int SY_PF = SY_OF + 8 * 64;
constexpr int SY_H2 = SY_PF + 32 * 64;
constexpr int SY_END = SY_H2 + 64 * 64;
static_assert(SY_END * 4 <= 131072, "sync area");
constexpr size_t WS_H = 131072;
constexpr size_t WS_H_BYTES = (size_t)64 * 288 * 1024;
constexpr size_t WS_QKV = WS_H + WS_H_BYTES;
constexpr size_t WS_QKV_BYTES = (size_t)3 * 32 * 2048 * 48;
constexpr size_t WS_O = WS_QKV + WS_QKV_BYTES + 4096;
constexpr size_t WS_O_BYTES = (size_t)128 * 18 * 1024;
constexpr size_t WS_P = WS_O + WS_O_BYTES;
constexpr size_t WS_P_BYTES = (size_t)8 * 4 * 8 * 8 * 9 * 1024;
constexpr size_t WS_X = WS_P + WS_P_BYTES;
constexpr size_t WS_X_BYTES = (size_t)64 * 36 * 1024;
constexpr size_t WS_USED = WS_X + WS_X_BYTES;

DI float lo2f(unsigned v) { return __uint_as_float(v << 16); }
DI float hi2f(unsigned v) { return __uint_as_float(v & 0xffff0000u); }
DI unsigned pk2(float a, float b) { f32x2 t = {a, b}; return __builtin_bit_cast(unsigned, __builtin_convertvector(t, bf16x2)); }
DI float rbf(float f) { return lo2f(pk2(f, 0.f)); }
DI float dot2(unsigned a, unsigned b, float c) { return __builtin_amdgcn_fdot2_f32_bf16(__builtin_bit_cast(bf16x2, a), __builtin_bit_cast(bf16x2, b), c, false); }
DI float xhalf_max(float v) { auto r = __builtin_amdgcn_permlane32_swap(__float_as_uint(v), __float_as_uint(v), false, false); return fmaxf(__uint_as_float(r[0]), __uint_as_float(r[1])); }
DI float lo_bcast(float v) { auto r = __builtin_amdgcn_permlane32_swap(__float_as_uint(v), __float_as_uint(v), false, false); return __uint_as_float(r[0]); }

DI void glds16(const void* sbase, unsigned voff, unsigned lds_dst) {
    asm volatile("s_mov_b32 m0, %2\n\ts_nop 0\n\tglobal_load_lds_dwordx4 %0, %1" ::"v"(voff), "s"(sbase), "s"(lds_dst) : "memory");
}
DI void glds16x3(const void* sbase, unsigned v0, unsigned v1, unsigned v2, unsigned lds_dst) {
    asm volatile("s_mov_b32 m0, %4\n\ts_nop 0\n\tglobal_load_lds_dwordx4 %0, %3\n\tglobal_load_lds_dwordx4 %1, %3 offset:1024\n\tglobal_load_lds_dwordx4 %2, %3 offset:2048"
                 ::"v"(v0), "v"(v1), "v"(v2), "s"(sbase), "s"(lds_dst) : "memory");
}
DI void glds16x4(const void* sbase, const unsigned (&v)[4], unsigned lds_dst) {
    asm volatile("s_mov_b32 m0, %5\n\ts_nop 0\n\tglobal_load_lds_dwordx4 %0, %4\n\tglobal_load_lds_dwordx4 %1, %4 offset:1024\n\tglobal_load_lds_dwordx4 %2, %4 offset:2048\n\tglobal_load_lds_dwordx4 %3, %4 offset:3072"
                 ::"v"(v[0]), "v"(v[1]), "v"(v[2]), "v"(v[3]), "s"(sbase), "s"(lds_dst) : "memory");
}
template <int OFF> DI void gload16_sc1(bf16x8& dst, unsigned voff, const char* sbase) {
    asm volatile("global_load_dwordx4 %0, %1, %2 offset:%3 sc1" : "=v"(dst) : "v"(voff), "s"(sbase), "i"(OFF) : "memory");
}
DI void gload4_sc1(unsigned& dst, unsigned voff, const void* sbase) {
    asm volatile("global_load_dword %0, %1, %2 sc1" : "=v"(dst) : "v"(voff), "s"(sbase) : "memory");
}
template <int N> DI void wait_vm_dep(bf16x8& v) { asm volatile("s_waitcnt vmcnt(%1)" : "+v"(v) : "i"(N)); }
template <int N> DI void wait_vm_dep2(bf16x8& v, bf16x8& w) { asm volatile("s_waitcnt vmcnt(%2)" : "+v"(v), "+v"(w) : "i"(N)); }
template <int N> DI void wait_vm_u(unsigned& v) { asm volatile("s_waitcnt vmcnt(%1)" : "+v"(v) : "i"(N)); }
template <int N> DI void wait_vm() { asm volatile("s_waitcnt vmcnt(%0)" ::"i"(N) : "memory"); }
#define WAIT_LGKM0() asm volatile("s_waitcnt lgkmcnt(0)" ::: "memory")
#define BAR() do { asm volatile("s_waitcnt lgkmcnt(0)" ::: "memory"); __builtin_amdgcn_s_barrier(); asm volatile("" ::: "memory"); } while (0)
#define BBAR() do { asm volatile("" ::: "memory"); __builtin_amdgcn_s_barrier(); asm volatile("" ::: "memory"); } while (0)
#define SB() __builtin_amdgcn_sched_barrier(0)
template <int... Is, class Fn> DI void sfor_impl(std::integer_sequence<int, Is...>, Fn&& f) { (f(std::integral_constant<int, Is>{}), ...); }
template <int N, class Fn> DI void sfor(Fn&& f) { sfor_impl(std::make_integer_sequence<int, N>{}, f); }
template <int V> using IC = std::integral_constant<int, V>;
DI void pin(bf16x8& v) { asm volatile("" : "+v"(v)); }
DI float glu_a(float g) { asm volatile("v_mul_f32 %0, 0xbfb8aa3b, %0\n\tv_exp_f32 %0, %0" : "+v"(g)); return g; }
DI float glu_b(float e, float u) { asm volatile("v_add_f32 %0, 1.0, %0\n\tv_rcp_f32 %0, %0\n\ts_nop 0\n\tv_mul_f32 %0, %0, %1" : "+v"(e) : "v"(u)); return e; }
template <int X, int ADD> DI unsigned xor_add(unsigned base) {
    unsigned r; asm volatile("v_xor_b32 %0, %2, %1\n\tv_add_u32 %0, %3, %0" : "=&v"(r) : "v"(base), "i"(X), "i"(ADD)); return r;
}
DI unsigned opaque(unsigned v) { asm volatile("" : "+v"(v)); return v; }
DI int fresh_lane() { int l; asm volatile("v_mbcnt_lo_u32_b32 %0, -1, 0\n\tv_mbcnt_hi_u32_b32 %0, -1, %0" : "=v"(l)); return l; }
DI bf16x8 tr_frag_o(unsigned base, int off0, int off1) {
    s16x4 lo = __builtin_amdgcn_ds_read_tr16_b64_v4i16((LAS s16x4*)((LAS char*)(size_t)base + off0));
    s16x4 hi = __builtin_amdgcn_ds_read_tr16_b64_v4i16((LAS s16x4*)((LAS char*)(size_t)base + off1));
    s16x8 f = __builtin_shufflevector(lo, hi, 0, 1, 2, 3, 4, 5, 6, 7);
    return __builtin_bit_cast(bf16x8, f);
}
DI u32x4 lds_rd128(unsigned a) { return *(LAS u32x4*)(size_t)a; }
DI unsigned lds_rd32(unsigned a) { return *(LAS unsigned*)(size_t)a; }
#define MFMA32(a, b, c) __builtin_amdgcn_mfma_f32_32x32x16_bf16((a), (b), (c), 0, 0, 0)
#define MFMA16(a, b, c) __builtin_amdgcn_mfma_f32_16x16x32_bf16((a), (b), (c), 0, 0, 0)

typedef __amdgpu_buffer_rsrc_t rsrc_t;
DI void st16_sc1(rsrc_t r, unsigned off, u32x4 v) { __builtin_amdgcn_raw_buffer_store_b128(v, r, off, 0, 16); }
DI void st16_sc1s(rsrc_t r, unsigned voff, unsigned soff, u32x4 v) {
    asm volatile("buffer_store_dwordx4 %0, %1, %2, %3 offen sc1\n\ts_nop 1" ::"v"(v), "v"(voff), "s"(r), "s"(soff) : "memory");
}
DI void st4_sc1(rsrc_t r, unsigned off, unsigned v) { __builtin_amdgcn_raw_buffer_store_b32(v, r, off, 0, 16); }
DI void flag_store(unsigned* sy, int idx, unsigned tag) { __hip_atomic_store((GAS unsigned*)sy + idx, tag, __ATOMIC_RELAXED, __HIP_MEMORY_SCOPE_AGENT); }
DI unsigned flag_load(const unsigned* sy, int idx) { return __hip_atomic_load((GAS unsigned*)sy + idx, __ATOMIC_RELAXED, __HIP_MEMORY_SCOPE_AGENT); }
constexpr unsigned kSpinCap = 1u << 20;
DI void wait_flags(const unsigned* sy, int idx, unsigned tag) {
    for (unsigned spins = 0; spins < kSpinCap; ++spins) {
        const unsigned v = flag_load(sy, idx);
        if (__all(v == tag)) break;
        __builtin_amdgcn_s_sleep(1);
    }
    asm volatile("" ::: "memory");
}

#define STAMP(i) do {} while (0)

template <int CTRL, int RMASK> DI float dpp_add(float v) {
    return v + __int_as_float(__builtin_amdgcn_update_dpp(0, __float_as_int(v), CTRL, RMASK, 0xf, true));
}
DI float wave_sum(float v) {
    v = dpp_add<0x111, 0xf>(v); v = dpp_add<0x112, 0xf>(v); v = dpp_add<0x114, 0xf>(v); v = dpp_add<0x118, 0xf>(v);
    v = dpp_add<0x142, 0xa>(v); v = dpp_add<0x143, 0xc>(v);
    return __int_as_float(__builtin_amdgcn_readlane(__float_as_int(v), 63));
}

constexpr int SLOT = 24576;
constexpr int SPARE = SLOT * 6;
constexpr int LDS_BYTES = 163840;
constexpr int KPOS = 4 * SLOT, VPOS = 5 * SLOT;
constexpr int WSL = 27648;
constexpr int WQ0 = 0, WQ1 = WSL, WQ2 = 2 * WSL, WQ3 = 3 * WSL, WQ4 = 4 * WSL, OTOP = 5 * WSL;
static_assert(WQ3 <= KPOS && OTOP + 18 * 1024 <= LDS_BYTES, "lds");

struct Ctx {
    const char* wq; const char* wgu;
    unsigned vq0, vq1, vq2, vg0, vg1, vg2;
    unsigned ldsbase;
    int wave; int P0;
};
DI void issue_q(const Ctx& c, int chunk, int pos) { glds16x3(c.wq + (size_t)(chunk * 192) * 3456, c.vq0, c.vq1, c.vq2, c.ldsbase + pos * SLOT + c.wave * 3072); }
DI void issue_gu(const Ctx& c, int panel, int chunk, int pos) {
    glds16x3(c.wgu + (size_t)(chunk * 96) * 9216 + (size_t)(c.P0 + panel) * 128, c.vg0, c.vg1, c.vg2, c.ldsbase + pos * SLOT + c.wave * 3072);
}

constexpr int PF1 = 3;
DI void issue_slot(const Ctx& c, int m) {
    if (m < 6) issue_gu(c, 1, m, m % 6);
    else if (m < 9) issue_q(c, m - 6, m % 6);
    else if (m < 15) issue_gu(c, 0, m - 9, m % 6);
    else issue_gu(c, 2, m - 15, m % 6);
}

struct Hand {
    unsigned* sy; unsigned tag; rsrc_t rs; int tt, j, c4, cgrp;
    const char* kb; const char* vb; const char* qb;
};

constexpr int NSTEPS = 396;
struct Step { short slot, ks, t, set, tau, first, fmt; };
struct Sched {
    Step st[NSTEPS];
    short fill[NSTEPS + 100][3];
    short nrows;
    short qn;
    short qent[24][5];
    short qlast;
};
template <int C4> constexpr Sched make_sched() {
    Sched S{};
    const int NTa = 4 - C4, NTc = 1 + C4;
    int setsA[4] = {2, 3, 0, 1};
    int setsC[4] = {0, 0, 0, 0};
    {
        int k = 0;
        for (int s = 0; s < 4; ++s) { int order[4] = {3, 0, 1, 2}; int cand = order[s]; bool used = false; for (int t = 0; t < NTa; ++t) used |= (setsA[t] == cand); if (!used && k < NTc - 1) setsC[k++] = cand; }
        setsC[NTc - 1] = setsA[0];
    }
    int n = 0, lastStep[11] = {};
    for (int slot = 0; slot < 21; ++slot) {
        int pass = slot < 6 ? 0 : slot < 9 ? 1 : slot < 15 ? 2 : 3;
        int i = pass == 0 ? slot : pass == 1 ? slot - 6 : pass == 2 ? slot - 9 : slot - 15;
        int nt = pass == 0 ? 4 : pass == 1 ? 2 : pass == 2 ? NTa : NTc;
        int kps = pass == 1 ? 12 : 6;
        int nsl = pass == 1 ? 3 : 6;
        int tau0 = pass == 0 ? 0 : pass == 1 ? 4 : pass == 2 ? 6 : 6 + NTa;
        bool tmajor = (i == nsl - 1) || (i == 0 && pass > 0);
        for (int k = 0; k < nt * kps; ++k) {
            int t = tmajor ? k / kps : k % nt, s = tmajor ? k % kps : k / nt;
            Step e{};
            e.slot = slot; e.ks = i * kps + s; e.t = t; e.tau = tau0 + t; e.fmt = pass == 1; e.first = (e.ks == 0);
            e.set = pass == 0 ? t : pass == 1 ? t : pass == 2 ? setsA[t] : setsC[t];
            S.st[n] = e; lastStep[e.tau] = n; ++n;
        }
    }
    const int delta = C4 == 0 ? 0 : C4 == 1 ? 6 : C4 == 2 ? 4 : 2;
    int qn = 0;
    for (int sl = 0; sl < 3; ++sl)
        for (int t = 0; t < 2; ++t)
            for (int r = 0; r < 16; r += 2) {
                int m = 0, d0 = 0;
                for (int hh = 1; hh >= 0; --hh) { int nn = 32 * t + (r & 3) + 8 * (r >> 2) + 4 * hh - delta; if (nn >= 0 && nn < 54 && nn / 18 == sl) { m |= 1 << hh; d0 = nn % 18 - 4 * hh; } }
                if (m) { S.qent[qn][0] = t; S.qent[qn][1] = r; S.qent[qn][2] = sl; S.qent[qn][3] = m; S.qent[qn][4] = d0; ++qn; }
            }
    S.qn = qn;
    int freeg = 0;
    auto put = [&](int g, int code) { for (int k = 0; k < 3; ++k) if (S.fill[g][k] == 0) { S.fill[g][k] = code; return; } S.fill[g][0] = -1; };
    for (int tau = 0; tau < 11; ++tau) {
        if (tau == 4) continue;
        int ready = lastStep[tau] + 3;
        int g = ready > freeg ? ready : freeg;
        int dl = 100000;
        for (int k = lastStep[tau] + 1; k < NSTEPS; ++k) if (S.st[k].first && (S.st[k].set == S.st[lastStep[tau]].set || (tau == 5 && S.st[k].set == S.st[lastStep[4]].set))) { dl = k; break; }
        int ops[40] = {}, cnt = 0;
        if (tau == 5) {
            for (int sl = 0; sl < 3; ++sl) {
                for (int e = 0; e < qn; ++e) if (S.qent[e][2] == sl) ops[cnt++] = 2000 + e;
                ops[cnt++] = 2100 + sl; ops[cnt++] = 2110 + sl;
            }
        } else {
            const int b = 1000 + tau * 32;
            ops[cnt++] = b + 0;
            for (int j = 0; j < 7; ++j) { ops[cnt++] = b + j + 1; ops[cnt++] = b + 8 + j; }
            ops[cnt++] = b + 15; ops[cnt++] = b + 16; ops[cnt++] = b + 17;
        }
        int avail = dl - g, dbl = cnt > avail ? cnt - avail : 0;
        if (tau == 5 && dbl < cnt / 2) dbl = cnt / 2;
        int k = 0;
        while (k < cnt) { put(g, ops[k++]); if (dbl > 0 && k < cnt) { put(g, ops[k++]); --dbl; } ++g; }
        if (tau == 5) { for (int q = 0; q < NSTEPS + 100; ++q) for (int z = 0; z < 3; ++z) if (S.fill[q][z] == 2112) S.qlast = q; }
        freeg = g;
    }
    S.nrows = freeg;
    return S;
}
template <int C4> struct SchedOf { static constexpr Sched SC = make_sched<C4>(); };
template <int C4> constexpr int set_of(int tau) { for (int g = 0; g < NSTEPS; ++g) if (SchedOf<C4>::SC.st[g].tau == tau) return SchedOf<C4>::SC.st[g].set; return 0; }
constexpr int XLA = 16;
constexpr int xwait(int s) {
    int n = (s + XLA > 35 ? 35 : s + XLA) - s;
    for (int sl = 1; sl <= 5; ++sl) if (s - XLA < 6 * sl && 6 * sl <= s) n += 3;
    return n;
}

template <int C4>
DI void gemm1(const Ctx& c, const Hand& hd, char* ws, int tile32) {
#define S (SchedOf<C4>::SC)
    constexpr int NTa = 4 - C4;
    constexpr int NK = (C4 <= 1) ? 17 : (C4 == 2 ? 18 : 19), NV = (C4 <= 2) ? 18 : 19, NCHK = NK - 1;
    constexpr int NQF = S.st[S.qlast < NSTEPS ? S.qlast : NSTEPS - 1].slot + 4;
    static_assert(NQF <= 16, "qkv publish");
    unsigned Lgu, Lq = 0, l16;
    {
        const int lane = fresh_lane();
        const int hh = lane >> 5, blk = (lane >> 4) & 1, q = (lane & 15) >> 2, pp = lane & 3;
        Lgu = c.ldsbase + (unsigned)(blk * 12288 + (8 * hh + q) * 128 + 8 * pp + 32 * (((q >> 1) & 1) | (blk << 1)));
        l16 = lane * 16;
    }
    const unsigned hbs = (unsigned)WS_H + ((unsigned)tile32 * 288u + (unsigned)c.P0 * 4u) * 1024u;
    const unsigned stg = c.ldsbase + SPARE + c.wave * 2048;
    const unsigned qds = (unsigned)WS_QKV + (unsigned)((hd.cgrp * 3) * 2048 + tile32 * 32) * 48u;
    unsigned stw = 0;
    const char* xs = ws + WS_X + (size_t)tile32 * 36 * 1024;
    bf16x8 xf[36];
    f32x16 acc[4];
    unsigned cur[4];
    bf16x8 A[PF1 + 1];
    unsigned qflag = 0, pl0 = 0, pl1 = 0;
    sfor<XLA>([&](auto SI) { constexpr int s = SI; gload16_sc1<(s & 3) * 1024>(xf[s], l16, xs + (s >> 2) * 4096); });
    auto rd = [&](auto IDX) {
        constexpr Step e = S.st[IDX];
        constexpr int kl = e.fmt ? e.ks % 12 : e.ks % 6, pos = e.slot % 6;
        constexpr int col = e.fmt ? e.t : (e.slot >= 9 && e.slot < 15 ? C4 + e.t : e.t);
        if constexpr (e.fmt && e.ks == 0 && e.t == 0) {
            const int lane = fresh_lane();
            const int hh = lane >> 5, blk = (lane >> 4) & 1, q = (lane & 15) >> 2, pp = lane & 3;
            Lq = c.ldsbase + (unsigned)((8 * hh + q) * 128 + 8 * pp + 32 * (blk ^ (((q >> 1) & 1) << 1)));
        }
        if constexpr (kl == 0) cur[e.t] = e.fmt ? xor_add<(col << 6), pos * SLOT>(Lq) : xor_add<(col << 5), pos * SLOT>(Lgu);
        return tr_frag_o(cur[e.t], kl * 2048, kl * 2048 + 512);
    };
    auto filler = [&](auto CODE) {
        constexpr int code = CODE;
        if constexpr (code >= 1000 && code < 2000) {
            constexpr int tau = (code - 1000) / 32, j = (code - 1000) % 32;
            constexpr int realset = set_of<C4>(tau);
            constexpr int fgo = tau < 4 ? 4 + tau : (tau < 6 + NTa ? C4 + tau - 6 : 8 + tau - 6 - NTa);
            if constexpr (j < 8) acc[realset][j] = glu_a(acc[realset][j]);
            else if constexpr (j < 16) acc[realset][j - 8] = glu_b(acc[realset][j - 8], acc[realset][j]);
            else if constexpr (j == 16) {
                pl0 = pk2(acc[realset][0], acc[realset][1]);
                pl1 = pk2(acc[realset][2], acc[realset][3]);
            } else {
                u32x4 w = {pl0, pl1, pk2(acc[realset][4], acc[realset][5]), pk2(acc[realset][6], acc[realset][7])};
                st16_sc1s(hd.rs, l16, hbs + fgo * 1024, w);
            }
        } else if constexpr (code >= 2000 && code < 2100) {
            constexpr int e = code - 2000, t = S.qent[e][0], r = S.qent[e][1], m = S.qent[e][3], d0 = S.qent[e][4];
            if constexpr (e == 0) { const int ln = fresh_lane(); stw = opaque(stg + (ln & 31) * 48 + ((ln >> 5) << 3)); }
            const float qs = (hd.cgrp * 3 + S.qent[e][2] < 32) ? 0.23570226039551584f * 1.4426950408889634f : 1.f;
            const unsigned v = pk2(acc[t][r] * qs, acc[t][r + 1] * qs);
            if constexpr (m == 3) *(LAS unsigned*)(size_t)(stw + 2 * d0) = v;
            else if ((int)((stw >> 3) & 1) == (m >> 1)) *(LAS unsigned*)(size_t)(stw + 2 * d0) = v;
        } else if constexpr (code >= 2100 && code < 2110) {
            constexpr int sl = code - 2100;
            if (((stw >> 3) & 1) == 0) *(LAS unsigned*)(size_t)(stw + 36) = (hd.cgrp * 3 + sl >= 64) ? 0x00003F80u : 0u;
            else *(LAS u32x2*)(size_t)(stw + 32) = (u32x2){0u, 0u};
        } else if constexpr (code >= 2110) {
            constexpr int sl = code - 2110;
            const u32x4 w0 = lds_rd128(stg + l16);
            st16_sc1s(hd.rs, l16, qds + (unsigned)sl * 2048u * 48u, w0);
            if (fresh_lane() < 32) { const u32x4 w1 = lds_rd128(stg + 1024 + l16); st16_sc1s(hd.rs, l16, qds + (unsigned)sl * 2048u * 48u + 1024, w1); }
        }
    };
    sfor<NSTEPS>([&](auto IDX) {
        constexpr int idx = IDX;
        constexpr Step e = S.st[idx];
        constexpr bool top = (idx == 0) || (S.st[idx > 0 ? idx - 1 : 0].slot != e.slot);
        if constexpr (top) {
            constexpr int n = e.slot;
            if constexpr (n == 5) {
                wait_vm<3>();
                pin(xf[30]); pin(xf[31]); pin(xf[32]); pin(xf[33]); pin(xf[34]); pin(xf[35]);
            } else if constexpr (n >= 6 && n <= 16) wait_vm<9>();
            else if constexpr (n == 17) wait_vm<6>();
            else if constexpr (n == 18) wait_vm<3 + (NK <= 17 ? 3 : 0)>();
            else if constexpr (n == 19) wait_vm<(NK <= 18 ? 3 : 0) + (NV <= 18 ? 5 : 0)>();
            if constexpr (n == NCHK) {
                if (c.wave == 0) {
                    wait_vm_u<(n == 16 ? 3 : 0)>(qflag);
                    if (!__all(qflag == hd.tag)) { const int ln = fresh_lane(); wait_flags(hd.sy, SY_QF + ((ln >= 32 && hd.tt > 0) ? hd.tt - 1 : hd.tt) * 64 + (ln & 31), hd.tag); }
                    asm volatile("buffer_inv sc1" ::: "memory");
                }
            }
            if constexpr (n == NK) { if (c.wave == 0) wait_vm<0>(); }
            if constexpr (n > 0) BBAR();
            if constexpr (n == NQF) { if (c.wave == 0 && fresh_lane() == 0) flag_store(hd.sy, SY_QF + hd.tt * 64 + hd.cgrp, hd.tag); }
            if constexpr (n == NCHK - 1) { if (c.wave == 0) { const int lane = fresh_lane(); gload4_sc1(qflag, (unsigned)(SY_QF + ((lane >= 32 && hd.tt > 0) ? hd.tt - 1 : hd.tt) * 64 + (lane & 31)) * 4u, hd.sy); } }
            if constexpr (n >= 1 && n + 5 <= 20) issue_slot(c, n + 5);
            if constexpr (n == NK || n == NV) {
#pragma unroll
                for (int kv = 0; kv < 2; ++kv) {
                    if ((kv == 0 && n == NK) || (kv == 1 && n == NV)) {
#pragma unroll
                        for (int k = 0; k < 3; ++k) {
                            const int m = c.wave * 3 + k;
                            int off = (hd.tt * 256 - 256) * 48 + m * 1024;
                            if (off < 0) off = m * 1024;
                            glds16((kv == 0 ? hd.kb : hd.vb) + off, l16, c.ldsbase + (kv == 0 ? KPOS : VPOS) + m * 1024);
                        }
                    }
                }
            }
            if constexpr (n == NV) {
#pragma unroll
                for (int k = 0; k < 2; ++k) {
                    int m = c.wave * 2 + k; if (m > 11) m = 11;
                    glds16(hd.qb + m * 1024, l16, c.ldsbase + SPARE + m * 1024);
                }
            }
            if constexpr (idx == 0) sfor<PF1>([&](auto J) { A[J] = rd(J); });
        }
        if constexpr (idx + PF1 < NSTEPS) A[(idx + PF1) % (PF1 + 1)] = rd(IC<idx + PF1>{});
        if constexpr (e.slot < 6 && e.t == 0 && e.slot < 5) {
            if constexpr (e.ks + XLA < 36) gload16_sc1<((e.ks + XLA) & 3) * 1024>(xf[e.ks + XLA], l16, xs + ((e.ks + XLA) >> 2) * 4096);
            wait_vm_dep<xwait(e.ks)>(xf[e.ks]);
        }
        SB();
        if constexpr (e.first) {
            f32x16 z;
#pragma unroll
            for (int r = 0; r < 16; ++r) z[r] = 0.f;
            acc[e.set] = MFMA32(A[idx % (PF1 + 1)], xf[e.ks], z);
        } else acc[e.set] = MFMA32(A[idx % (PF1 + 1)], xf[e.ks], acc[e.set]);
        SB();
        if constexpr (S.fill[idx][0] != 0) {
            filler(IC<S.fill[idx][0]>{});
            if constexpr (S.fill[idx][1] != 0) filler(IC<S.fill[idx][1]>{});
            if constexpr (S.fill[idx][2] != 0) filler(IC<S.fill[idx][2]>{});
            SB();
        }
    });
    sfor<S.nrows - NSTEPS>([&](auto R) {
        constexpr int g = NSTEPS + R;
        if constexpr (S.fill[g][0] != 0) filler(IC<S.fill[g][0]>{});
        if constexpr (S.fill[g][1] != 0) filler(IC<S.fill[g][1]>{});
        if constexpr (S.fill[g][2] != 0) filler(IC<S.fill[g][2]>{});
    });
}
#undef S

constexpr int g2_ks(int u) { constexpr int o[18] = {0, 1, 2, 3, 5, 6, 7, 9, 10, 11, 14, 15, 4, 8, 12, 13, 16, 17}; return o[u]; }
constexpr int g2_place(int sl) { return sl == 0 ? WQ0 : sl == 1 ? WQ1 : sl == 2 ? WQ2 : sl == 3 ? WQ3 : sl == 4 ? WQ4 : WQ0; }
constexpr int ewait(int u) { constexpr int c[8] = {0, 4, 8, 16, 16, 20, 24, 24}; return 2 * (11 - u) + 2 + c[u]; }

__global__ __launch_bounds__(512, 2) void mega_kernel(const u16* __restrict__ x, const u16* __restrict__ wqkv, const u16* __restrict__ wo,
                                                      const u16* __restrict__ wg, const u16* __restrict__ wu, const u16* __restrict__ wd,
                                                      char* __restrict__ ws, u16* __restrict__ out) {
    extern __shared__ __attribute__((aligned(16))) char smem[];
    const int tid = threadIdx.x;
    const int wave = __builtin_amdgcn_readfirstlane(tid >> 6);
    const int bid = blockIdx.x, xj = bid & 7, loc = bid >> 3;
    const int c4 = loc & 3, tt = loc >> 2, cgrp = xj * 4 + c4;
    const unsigned ldsbase = (unsigned)(size_t)(LAS char*)smem;
    unsigned* sy = (unsigned*)(ws + WS_SYNC);
    const rsrc_t rs = __builtin_amdgcn_make_buffer_rsrc(ws, 0, (unsigned)WS_USED, 0x00020000);
    unsigned tag;
    Ctx c;

    STAMP(0);
    {
        const int lane = tid & 63;
        const int xrow = bid * 8 + wave;
        const char* xr = (const char*)x + (size_t)xrow * 1152;
        u32x4 xv0 = *(const u32x4*)(xr + lane * 16);
        u32x4 xv1 = *(const u32x4*)(xr + 1024 + (lane & 7) * 16);
        const unsigned gen = flag_load(sy, SY_GEN);
        const int qa0 = (108 * cgrp) & ~15;
        c.wq = (const char*)wqkv + qa0; c.wgu = (const char*)((wave >> 2) ? wu : wg);
        c.ldsbase = ldsbase; c.wave = wave; c.P0 = (9 * cgrp) >> 2;
        {
            const int lrow = lane >> 3, c16 = lane & 7, bsel = wave >> 2;
            const int swgu = ((lrow >> 1) & 1) | (bsel << 1);
            const int chgu = (((c16 >> 1) ^ swgu) << 1) | (c16 & 1);
            c.vg0 = (unsigned)((24 * (wave & 3) + lrow) * 9216 + chgu * 16); c.vg1 = c.vg0 + 8 * 9216 - 1024; c.vg2 = c.vg0 + 16 * 9216 - 2048;
            const int swq = ((lrow >> 1) & 1) << 1;
            int chq = (((c16 >> 1) ^ swq) << 1) | (c16 & 1);
            if (cgrp == 31 && chq == 7) chq = 6;
            c.vq0 = (unsigned)((24 * wave + lrow) * 3456 + chq * 16); c.vq1 = c.vq0 + 8 * 3456 - 1024; c.vq2 = c.vq0 + 16 * 3456 - 2048;
        }
        tag = (unsigned)__builtin_amdgcn_readfirstlane((int)(gen * 0x9E3779B1u + 0x7F4A7C15u));
        float s1 = 0.f;
        if (lane >= 8) xv1 = (u32x4){0u, 0u, 0u, 0u};
#pragma unroll
        for (int k = 0; k < 4; ++k) { s1 = dot2(xv0[k], 0x3f803f80u, s1); s1 = dot2(xv1[k], 0x3f803f80u, s1); }
        const float meanf = wave_sum(s1) * (1.f / 576.f);
        float s2 = 0.f;
#pragma unroll
        for (int k = 0; k < 4; ++k) {
            float a = lo2f(xv0[k]) - meanf, b = hi2f(xv0[k]) - meanf; s2 += a * a + b * b;
            if (lane < 8) { a = lo2f(xv1[k]) - meanf; b = hi2f(xv1[k]) - meanf; s2 += a * a + b * b; }
        }
        const float var = rbf(wave_sum(s2) * (1.f / 576.f));
        const float mean = rbf(meanf);
        const float rden = 1.f / rbf(sqrtf(rbf(var + 1e-5f)));
        u32x4 o0, o1;
#pragma unroll
        for (int k = 0; k < 4; ++k) {
            o0[k] = pk2(rbf(lo2f(xv0[k]) - mean) * rden, rbf(hi2f(xv0[k]) - mean) * rden);
            o1[k] = pk2(rbf(lo2f(xv1[k]) - mean) * rden, rbf(hi2f(xv1[k]) - mean) * rden);
        }
        *(LAS u32x4*)(size_t)(ldsbase + SPARE + (lane * 8 + wave) * 16) = o0;
        if (lane < 8) *(LAS u32x4*)(size_t)(ldsbase + SPARE + ((64 + lane) * 8 + wave) * 16) = o1;
        BAR();
        const unsigned xd = (unsigned)WS_X + (unsigned)(bid >> 2) * 36u * 1024u;
        const int t0 = (bid & 3) * 8;
        {
            const int i = tid, cc = i >> 3, tk = i & 7;
            st16_sc1(rs, xd + ((cc >> 1) * 64 + (cc & 1) * 32 + t0 + tk) * 16, lds_rd128(ldsbase + SPARE + i * 16));
        }
        {
            const int i = 512 + lane, cc = i >> 3, tk = i & 7;
            st16_sc1(rs, wave == 0 ? xd + ((cc >> 1) * 64 + (cc & 1) * 32 + t0 + tk) * 16 : 0xFFFFFF00u, lds_rd128(ldsbase + SPARE + i * 16));
        }
#pragma unroll
        for (int m = 0; m < 6; ++m) issue_slot(c, m);
        wait_vm<18>();
        BBAR();
        if (tid == 0) { flag_store(sy, SY_XF + (bid >> 5) * 64 + (bid & 31), tag); flag_store(sy, SY_START + bid, tag); }
    }
    STAMP(1);
    Hand hd;
    hd.sy = sy; hd.tag = tag; hd.rs = rs; hd.tt = tt; hd.j = xj; hd.c4 = c4; hd.cgrp = cgrp;
    hd.kb = ws + WS_QKV + ((size_t)(32 + cgrp) * 2048) * 48; hd.vb = ws + WS_QKV + ((size_t)(64 + cgrp) * 2048) * 48;
    hd.qb = ws + WS_QKV + ((size_t)cgrp * 2048 + tt * 256) * 48;

    if (wave == 0) wait_flags(sy, SY_XF + tt * 64 + (fresh_lane() & 31), tag);
    wait_vm<0>();
    BBAR();
    STAMP(2);
    const int tile32 = tt * 8 + wave;
    if (c4 == 0) gemm1<0>(c, hd, ws, tile32);
    else if (c4 == 1) gemm1<1>(c, hd, ws, tile32);
    else if (c4 == 2) gemm1<2>(c, hd, ws, tile32);
    else gemm1<3>(c, hd, ws, tile32);
    STAMP(7);

    {
    const int lane = fresh_lane();
    const int hh = lane >> 5, blk = (lane >> 4) & 1, q = (lane & 15) >> 2, pp = lane & 3;
    const unsigned l16 = lane * 16;
    const bool tid0 = (wave == 0) && (lane == 0);
    const char* wdb = (const char*)wd + (size_t)(xj * 576) * 1152 + c4 * 288;
    const int mb = wave < 6 ? 4 * wave : 23;
    unsigned vo[4];
#pragma unroll
    for (int i = 0; i < 4; ++i) {
        const int n = (mb + i) * 64 + lane, row = n / 18, cc = n - row * 18;
        vo[i] = (unsigned)(row * 1152 + cc * 16 - 1024 * i);
    }
    auto issue_wd = [&](int slot, int dst) { glds16x4(wdb + (size_t)slot * 96 * 1152, vo, ldsbase + dst + mb * 1024); };
    const int q4 = lane >> 4, t16 = lane & 15, qq = (lane & 15) >> 2;
    const char* hsrc = ws + WS_H + ((size_t)tile32 * 288 + xj * 36) * 1024;
    const unsigned hvo = (unsigned)(((q4 >> 1) * 64 + (q4 & 1) * 32 + t16) * 16);
    bf16x8 hf[18][2];
    auto hload = [&](auto UI) { constexpr int u = UI, s = g2_ks(u);
        gload16_sc1<(s & 1) * 2048>(hf[u][0], hvo, hsrc + (s >> 1) * 4096);
        gload16_sc1<(s & 1) * 2048 + 256>(hf[u][1], hvo, hsrc + (s >> 1) * 4096); };
    unsigned hflag = 0;
    const int hfi = (xj * 8 + tt) * 64;
    {
        if (c4 == 0) wait_vm<1>(); else if (c4 == 1) wait_vm<2>(); else if (c4 == 2) wait_vm<3>(); else wait_vm<4>();
        BBAR();
        if (tid0) flag_store(sy, SY_HF + hfi + c4, tag);
        issue_wd(0, WQ0); issue_wd(1, WQ1); issue_wd(2, WQ2);
        STAMP(8);
        const int t = lane & 31;
        const int q0 = tt * 256 + wave * 32;
        bf16x8 Qf0, Qf1;
        {
            const unsigned qa = ldsbase + SPARE + (wave * 32 + t) * 48;
            Qf0 = __builtin_bit_cast(bf16x8, lds_rd128(qa + 16 * hh));
            const unsigned qv1 = lds_rd32(qa + 32);
            u32x4 q1 = {hh == 0 ? qv1 : 0u, 0u, 0u, 0u};
            Qf1 = __builtin_bit_cast(bf16x8, q1);
        }
        const unsigned kaddr = ldsbase + KPOS + (wave * 32 + t) * 48;
        const unsigned vaddr = ldsbase + VPOS + (wave * 32 + 4 * hh + q) * 48 + 32 * blk + 8 * pp;
        const float NEG = -__builtin_inff();
        f32x16 o;
        auto attend = [&](auto FULLC) {
            constexpr bool FULL = FULLC;
            const int ktmin = FULL ? 0 : 8 - wave;
            f32x16 sc[9];
#pragma unroll
            for (int kt = 0; kt < 9; ++kt) {
                if (FULL || kt >= ktmin) {
                    u32x4 k0 = lds_rd128(kaddr + kt * 1536 + 16 * hh);
                    u32x4 k1 = {lds_rd32(kaddr + kt * 1536 + 32), 0u, 0u, 0u};
                    f32x16 z;
#pragma unroll
                    for (int r = 0; r < 16; ++r) z[r] = 0.f;
                    z = MFMA32(__builtin_bit_cast(bf16x8, k0), Qf0, z);
                    sc[kt] = MFMA32(__builtin_bit_cast(bf16x8, k1), Qf1, z);
                } else {
#pragma unroll
                    for (int r = 0; r < 16; ++r) sc[kt][r] = NEG;
                }
            }
#pragma unroll
            for (int r = 0; r < 16; ++r) {
                const int kk = (r & 3) + 8 * (r >> 2) + 4 * hh;
                if (!(kk > t)) sc[0][r] = NEG;
                if (!(kk <= t)) sc[8][r] = NEG;
            }
            float m = NEG;
#pragma unroll
            for (int kt = 0; kt < 9; ++kt)
#pragma unroll
                for (int r = 0; r < 16; ++r) m = fmaxf(m, sc[kt][r]);
            m = xhalf_max(m);
            if (wave == 0) gload4_sc1(hflag, (unsigned)(SY_HF + hfi + (lane & 3)) * 4u, sy);
#pragma unroll
            for (int r = 0; r < 16; ++r) o[r] = 0.f;
            sfor<9>([&](auto KT) {
                constexpr int kt = KT;
                if constexpr (kt == 2) {
                    wait_vm<12>();
                    if (wave == 0) {
                        wait_vm_u<0>(hflag);
                        if (!__all(hflag == tag)) wait_flags(sy, SY_HF + hfi + (lane & 3), tag);
                    }
                    BBAR();
                    if (tid0) flag_store(sy, SY_H2 + hfi + c4, tag);
                }
                if constexpr (kt == 6) { if (wave == 0) gload4_sc1(hflag, (unsigned)(SY_H2 + hfi + (lane & 3)) * 4u, sy); }
                if (FULL || kt >= ktmin) {
                    unsigned pk[8];
#pragma unroll
                    for (int r = 0; r < 16; r += 2)
                        pk[r >> 1] = pk2(__builtin_amdgcn_exp2f(sc[kt][r] - m), __builtin_amdgcn_exp2f(sc[kt][r + 1] - m));
#pragma unroll
                    for (int s = 0; s < 2; ++s) {
                        u32x4 pb = {pk[4 * s], pk[4 * s + 1], pk[4 * s + 2], pk[4 * s + 3]};
                        bf16x8 A = tr_frag_o(vaddr, (kt * 32 + 16 * s) * 48, (kt * 32 + 16 * s + 8) * 48);
                        o = MFMA32(A, __builtin_bit_cast(bf16x8, pb), o);
                    }
                }
                if constexpr (kt >= 3) { SB(); hload(IC<2 * (kt - 3)>{}); hload(IC<2 * (kt - 3) + 1>{}); SB(); }
            });
        };
        if (tt > 0) attend(std::true_type{}); else attend(std::false_type{});
        const float inv = 1.f / lo_bcast(o[10]);
        const int tile16 = (q0 >> 4) + ((lane >> 4) & 1);
        const unsigned ob = (unsigned)WS_O + (unsigned)(tile16 * 18) * 1024u + t16 * 16;
        u32x4 w = {pk2(o[0] * inv, o[1] * inv), pk2(o[2] * inv, o[3] * inv), pk2(o[4] * inv, o[5] * inv), pk2(o[6] * inv, o[7] * inv)};
        st16_sc1(rs, ob + (cgrp >> 1) * 1024 + (2 * (cgrp & 1) + hh) * 256, w);
        st4_sc1(rs, hh == 0 ? ob + (16 + (cgrp >> 4)) * 1024 + ((cgrp >> 2) & 3) * 256 + (cgrp & 3) * 4 : 0xFFFFFFF0u, pk2(o[8] * inv, o[9] * inv));
        if (wave == 0) {
            wait_vm_u<14>(hflag);
            if (!__all(hflag == tag)) wait_flags(sy, SY_H2 + hfi + (lane & 3), tag);
        }
    }
    STAMP(9);
    f32x4 acc[2][9];
    const unsigned arel = ldsbase + (unsigned)((16 * (q4 >> 1) + 4 * (q4 & 1) + qq) * 288 + 8 * pp);
    const unsigned pdu = (unsigned)WS_P + (unsigned)(((tt * 4 + c4) * 8 + wave) * 8 + xj) * 9u * 1024u;
    {
        wait_vm<26>();
        BAR();
        constexpr int PF = 4;
        bf16x8 A[PF + 1];
        unsigned oflag = 0;
        constexpr int NS0 = 108, NST = 162;
        auto st_u = [](int idx) constexpr { return idx < NS0 ? idx / 9 : 12 + (idx - NS0) % 6; };
        auto st_dt = [](int idx) constexpr { return idx < NS0 ? idx % 9 : (idx - NS0) / 6; };
        unsigned cur = 0, curl[2] = {0, 0};
        auto rd = [&](auto IDX) {
            constexpr int idx = IDX, u = st_u(idx), dt = st_dt(idx);
            constexpr int ks = g2_ks(u), off = g2_place(ks / 3) + (ks % 3) * 32 * 288;
            if constexpr (idx < NS0) {
                if constexpr (dt == 0) cur = xor_add<0, off>(arel);
                return tr_frag_o(cur, dt * 32, dt * 32 + 8 * 288);
            } else {
                if constexpr (idx == NS0) { cur = xor_add<0, 0>(arel); curl[0] = xor_add<0, WQ4>(arel); curl[1] = xor_add<0, WQ2>(arel); }
                if constexpr (off >= WQ4) return tr_frag_o(curl[0], off - WQ4 + dt * 32, off - WQ4 + dt * 32 + 8 * 288);
                else if constexpr (off >= WQ2) return tr_frag_o(curl[1], off - WQ2 + dt * 32, off - WQ2 + dt * 32 + 8 * 288);
                else return tr_frag_o(cur, off + dt * 32, off + dt * 32 + 8 * 288);
            }
        };
        sfor<NST>([&](auto IDX) {
            constexpr int idx = IDX, u = st_u(idx), dt = st_dt(idx);
            if constexpr (idx < NS0 && dt == 0) {
                if constexpr (u == 0) STAMP(10);
                if constexpr (u == 3) {
                    wait_vm<12>();
                    BBAR();
                    if (tid0) flag_store(sy, SY_OF + tt * 64 + cgrp, tag);
                    issue_wd(5, WQ0);
                }
                if constexpr (u == 6) { wait_vm<12>(); BBAR(); }
                if constexpr (u == 8) {
                    wait_vm<8>();
                    sfor<4>([&](auto K) { pin(hf[8 + K][0]); pin(hf[8 + K][1]); });
                    BBAR();
                    if (wave == 0) gload4_sc1(oflag, (unsigned)(SY_OF + tt * 64 + (lane & 31)) * 4u, sy);
                }
                if constexpr (u == 10) {
                    if (wave == 0) {
                        wait_vm_u<0>(oflag);
                        if (!__all(oflag == tag)) wait_flags(sy, SY_OF + tt * 64 + (lane & 31), tag);
                        asm volatile("buffer_inv sc1" ::: "memory");
                    }
                }
                if constexpr (u == 11) {
                    wait_vm<0>();
                    sfor<6>([&](auto K) { pin(hf[12 + K][0]); pin(hf[12 + K][1]); });
                    BBAR();
                    const char* osrc = ws + WS_O + (size_t)((tt * 8 + xj) * 2) * 18 * 1024;
#pragma unroll
                    for (int i = 0; i < 5; ++i) {
                        int m = wave + 8 * i; if (m >= 36) m -= 8;
                        glds16(osrc + m * 1024, l16, ldsbase + (m < 18 ? OTOP + m * 1024 : WQ3 + (m - 18) * 1024));
                    }
                }
                if constexpr (idx == 0) sfor<PF>([&](auto J) { A[J] = rd(J); });
                if constexpr (u < 8) wait_vm_dep2<ewait(u)>(hf[u][0], hf[u][1]);
            }
            if constexpr (idx + PF < NST) A[(idx + PF) % (PF + 1)] = rd(IC<idx + PF>{});
            SB();
            if constexpr (u == 0) {
                acc[0][dt] = MFMA16(A[idx % (PF + 1)], hf[u][0], ((f32x4){0.f, 0.f, 0.f, 0.f}));
                acc[1][dt] = MFMA16(A[idx % (PF + 1)], hf[u][1], ((f32x4){0.f, 0.f, 0.f, 0.f}));
            } else {
                acc[0][dt] = MFMA16(A[idx % (PF + 1)], hf[u][0], acc[0][dt]);
                acc[1][dt] = MFMA16(A[idx % (PF + 1)], hf[u][1], acc[1][dt]);
            }
            SB();
            if constexpr (idx < NS0 && dt == 2) {
                if constexpr (u == 0) issue_wd(3, WQ3);
                if constexpr (u == 1) { hload(IC<12>{}); hload(IC<13>{}); }
                if constexpr (u == 2) issue_wd(4, WQ4);
                if constexpr (u == 4) { hload(IC<14>{}); hload(IC<15>{}); }
                if constexpr (u == 5) { hload(IC<16>{}); hload(IC<17>{}); }
                SB();
            }
            if constexpr (idx >= NS0 && u == 14 && dt > 0) {
                constexpr int d = dt - 1;
                u32x4 w = {pk2(acc[0][d][0], acc[0][d][1]), pk2(acc[0][d][2], acc[0][d][3]), pk2(acc[1][d][0], acc[1][d][1]), pk2(acc[1][d][2], acc[1][d][3])};
                st16_sc1s(rs, l16, pdu + d * 1024, w);
                SB();
            }
        });
        STAMP(16);
        u32x4 w = {pk2(acc[0][8][0], acc[0][8][1]), pk2(acc[0][8][2], acc[0][8][3]), pk2(acc[1][8][0], acc[1][8][1]), pk2(acc[1][8][2], acc[1][8][3])};
        st16_sc1s(rs, l16, pdu + 8 * 1024, w);
    }
    {
        unsigned vw[4], vw5[4];
#pragma unroll
        for (int i = 0; i < 4; ++i) {
            const int n = (mb + i) * 64 + lane, rs_ = n / 18, cc = n - rs_ * 18;
            const int u1 = rs_ >> 5, r1 = rs_ & 31, qx = 2 * (r1 >> 4) + ((r1 >> 2) & 1), tx = (r1 >> 3) & 1, qy = r1 & 3;
            const int f = 18 * (qx >> 1) + 4 * (qx & 1) + 8 * tx + qy;
            vw[i] = (unsigned)((36 * u1 + f) * 1152 + cc * 16 - 1024 * i);
            const int jx = 4 * tx + qy, g = 4 * (u1 - 1) + qx;
            const int row5 = (u1 == 0) ? 36 * 15 + f : 18 * (4 * g + (jx >> 1)) + 16 + (jx & 1);
            vw5[i] = (unsigned)(row5 * 1152 + cc * 16 - 1024 * i);
        }
        const char* wob = (const char*)wo + c4 * 288;
        auto issue_wo = [&](int slot, int dst) { if (slot < 5) glds16x4(wob + (size_t)slot * 108 * 1152, vw, ldsbase + dst + mb * 1024); else glds16x4(wob, vw5, ldsbase + dst + mb * 1024); };
        BAR();
        issue_wo(0, WQ0); issue_wo(1, WQ1); issue_wo(2, WQ2); issue_wo(3, WQ4);
        const int tok0 = tt * 256 + xj * 32;
        const unsigned psrc = (unsigned)WS_P + (unsigned)(((tt * 4 + c4) * 8 + xj) * 8) * 9u * 1024u + l16;
        auto fin = [&](auto NDC) {
            constexpr int ND = NDC;
            u32x2 xv[ND][2];
            u32x4 vy[ND][8];
            f32x4 sacc[ND][2];
            wait_vm<16>();
            BBAR();
            if (tid0) flag_store(sy, SY_PF + (tt * 4 + c4) * 64 + xj, tag);
            STAMP(17);
            auto sgroup = [&](auto G) {
                constexpr int g = G;
                sfor<6>([&](auto KK) {
                    constexpr int ks = 6 * g + KK, sl = ks / 3;
                    constexpr int place = sl == 0 ? WQ0 : sl == 1 ? WQ1 : sl == 2 ? WQ2 : sl == 3 ? WQ4 : sl == 4 ? WQ0 : WQ1;
                    const bf16x8 B0 = __builtin_bit_cast(bf16x8, lds_rd128(ldsbase + OTOP + ks * 1024 + l16));
                    const bf16x8 B1 = __builtin_bit_cast(bf16x8, lds_rd128(ldsbase + WQ3 + ks * 1024 + l16));
#pragma unroll
                    for (int k = 0; k < ND; ++k) {
                        const unsigned ab = arel + place + (ks % 3) * 32 * 288 + ((k == 0) ? wave : 8) * 32;
                        const bf16x8 A = tr_frag_o(ab, 0, 8 * 288);
                        if constexpr (ks == 0) { sacc[k][0] = MFMA16(A, B0, ((f32x4){0.f, 0.f, 0.f, 0.f})); sacc[k][1] = MFMA16(A, B1, ((f32x4){0.f, 0.f, 0.f, 0.f})); }
                        else { sacc[k][0] = MFMA16(A, B0, sacc[k][0]); sacc[k][1] = MFMA16(A, B1, sacc[k][1]); }
                    }
                });
            };
            wait_vm<8>();
            BBAR();
            sgroup(IC<0>{});
#pragma unroll
            for (int k = 0; k < ND; ++k) {
                const int dt = (k == 0) ? wave : 8;
#pragma unroll
                for (int a = 0; a < 2; ++a)
                    xv[k][a] = *(const u32x2*)((const char*)x + (size_t)(tok0 + a * 16 + t16) * 1152 + (c4 * 144 + dt * 16 + 4 * q4) * 2);
            }
            wait_flags(sy, SY_PF + (tt * 4 + c4) * 64 + (lane & 7), tag);
            STAMP(18);
#pragma unroll
            for (int k = 0; k < ND; ++k) {
                const int dt = (k == 0) ? wave : 8;
#pragma unroll
                for (int sj = 0; sj < 8; ++sj) vy[k][sj] = __builtin_amdgcn_raw_buffer_load_b128(rs, psrc + (sj * 9 + dt) * 1024, 0, 16);
            }
            BAR();
            issue_wo(4, WQ0); issue_wo(5, WQ1);
            sgroup(IC<1>{});
            wait_vm<0>();
            BBAR();
            sgroup(IC<2>{});
#pragma unroll
            for (int k = 0; k < ND; ++k) {
                const int dt = (k == 0) ? wave : 8;
                float y[8];
#pragma unroll
                for (int e = 0; e < 8; ++e) y[e] = 0.f;
#pragma unroll
                for (int sj = 0; sj < 8; ++sj)
#pragma unroll
                    for (int e = 0; e < 4; ++e) { y[2 * e] += lo2f(vy[k][sj][e]); y[2 * e + 1] += hi2f(vy[k][sj][e]); }
#pragma unroll
                for (int a = 0; a < 2; ++a) {
                    float ov[4];
#pragma unroll
                    for (int r = 0; r < 4; ++r) {
                        const float yy = rbf(y[4 * a + r]);
                        const float g = rbf(yy * __builtin_amdgcn_rcpf(1.f + __builtin_amdgcn_exp2f(-yy * 1.4426950408889634f)));
                        const float pr = rbf(rbf(sacc[k][a][r]) * g);
                        const unsigned xw = xv[k][a][r >> 1];
                        ov[r] = ((r & 1) ? hi2f(xw) : lo2f(xw)) + pr;
                    }
                    u32x2 w = {pk2(ov[0], ov[1]), pk2(ov[2], ov[3])};
                    *(u32x2*)((char*)out + (size_t)(tok0 + a * 16 + t16) * 1152 + (c4 * 144 + dt * 16 + 4 * q4) * 2) = w;
                }
            }
        };
        if (wave == 0) fin(IC<2>{}); else fin(IC<1>{});
        if (bid == 0 && wave == 7) {
            for (unsigned spins = 0; spins < kSpinCap; ++spins) {
                bool ok = true;
#pragma unroll
                for (int k = 0; k < 4; ++k) ok &= flag_load(sy, SY_START + lane + 64 * k) == tag;
                if (__all(ok)) break;
                __builtin_amdgcn_s_sleep(1);
            }
            if (lane == 0) flag_store(sy, SY_GEN, tag);
        }
    }
    }
    STAMP(19);
}

extern "C" void kernel_launch(void* const* d_in, const int* in_sizes, int n_in, void* d_out, int out_size, void* d_ws, size_t ws_size,
                              hipStream_t stream) {
    const u16* x = (const u16*)d_in[0];
    const u16* wqkv = (const u16*)d_in[1];
    const u16* wo = (const u16*)d_in[2];
    const u16* wg = (const u16*)d_in[4];
    const u16* wu = (const u16*)d_in[5];
    const u16* wd = (const u16*)d_in[6];
    (void)hipFuncSetAttribute((const void*)mega_kernel, hipFuncAttributeMaxDynamicSharedMemorySize, LDS_BYTES);
    mega_kernel<<<256, 512, LDS_BYTES, stream>>>(x, wqkv, wo, wg, wu, wd, (char*)d_ws, (u16*)d_out);
}
```

```cpp
#include <hip/hip_runtime.h>
#include <hip/hip_bf16.h>
#include <stdint.h>
#include <utility>

typedef unsigned short u16;
typedef __bf16 bf16x8 __attribute__((ext_vector_type(8)));
typedef __bf16 bf16x2 __attribute__((ext_vector_type(2)));
typedef float f32x16 __attribute__((ext_vector_type(16)));
typedef float f32x4 __attribute__((ext_vector_type(4)));
typedef float f32x2 __attribute__((ext_vector_type(2)));
typedef short s16x4 __attribute__((ext_vector_type(4)));
typedef short s16x8 __attribute__((ext_vector_type(8)));
typedef unsigned u32x4 __attribute__((ext_vector_type(4)));
typedef unsigned u32x2 __attribute__((ext_vector_type(2)));
#define LAS __attribute__((address_space(3)))
#define GAS __attribute__((address_space(1)))
#define DI __device__ __forceinline__

constexpr int S = 2048, D = 576, F = 4608;
constexpr size_t WS_SYNC = 0;
constexpr int SY_GEN = 0;
constexpr int SY_START = 64;
constexpr int SY_XF = 512;
constexpr int SY_QF = SY_XF + 8 * 64;
constexpr int SY_HF = SY_QF + 8 * 64;
constexpr int SY_OF = SY_HF + 64 * 64;
constexpr int SY_PF = SY_OF + 8 * 64;
constexpr int SY_H2 = SY_PF + 32 * 64;
constexpr int SY_END = SY_H2 + 64 * 64;
static_assert(SY_END * 4 <= 131072, "sync area");
constexpr size_t WS_H = 131072;
constexpr size_t WS_H_BYTES = (size_t)64 * 288 * 1024;
constexpr size_t WS_QKV = WS_H + WS_H_BYTES;
constexpr size_t WS_QKV_BYTES = (size_t)3 * 32 * 2048 * 48;
constexpr size_t WS_O = WS_QKV + WS_QKV_BYTES + 4096;
constexpr size_t WS_O_BYTES = (size_t)128 * 18 * 1024;
constexpr size_t WS_P = WS_O + WS_O_BYTES;
constexpr size_t WS_P_BYTES = (size_t)8 * 4 * 8 * 8 * 9 * 1024;
constexpr size_t WS_X = WS_P + WS_P_BYTES;
constexpr size_t WS_X_BYTES = (size_t)64 * 36 * 1024;
constexpr size_t WS_USED = WS_X + WS_X_BYTES;

DI float lo2f(unsigned v) { return __uint_as_float(v << 16); }
DI float hi2f(unsigned v) { return __uint_as_float(v & 0xffff0000u); }
DI unsigned pk2(float a, float b) { f32x2 t = {a, b}; return __builtin_bit_cast(unsigned, __builtin_convertvector(t, bf16x2)); }
DI float rbf(float f) { return lo2f(pk2(f, 0.f)); }
DI float dot2(unsigned a, unsigned b, float c) { return __builtin_amdgcn_fdot2_f32_bf16(__builtin_bit_cast(bf16x2, a), __builtin_bit_cast(bf16x2, b), c, false); }
DI float xhalf_max(float v) { auto r = __builtin_amdgcn_permlane32_swap(__float_as_uint(v), __float_as_uint(v), false, false); return fmaxf(__uint_as_float(r[0]), __uint_as_float(r[1])); }
DI float lo_bcast(float v) { auto r = __builtin_amdgcn_permlane32_swap(__float_as_uint(v), __float_as_uint(v), false, false); return __uint_as_float(r[0]); }

DI void glds16(const void* sbase, unsigned voff, unsigned lds_dst) {
    asm volatile("s_mov_b32 m0, %2\n\ts_nop 0\n\tglobal_load_lds_dwordx4 %0, %1" ::"v"(voff), "s"(sbase), "s"(lds_dst) : "memory");
}
DI void glds16x3(const void* sbase, unsigned v0, unsigned v1, unsigned v2, unsigned lds_dst) {
    asm volatile("s_mov_b32 m0, %4\n\ts_nop 0\n\tglobal_load_lds_dwordx4 %0, %3\n\tglobal_load_lds_dwordx4 %1, %3 offset:1024\n\tglobal_load_lds_dwordx4 %2, %3 offset:2048"
                 ::"v"(v0), "v"(v1), "v"(v2), "s"(sbase), "s"(lds_dst) : "memory");
}
DI void glds16x4(const void* sbase, const unsigned (&v)[4], unsigned lds_dst) {
    asm volatile("s_mov_b32 m0, %5\n\ts_nop 0\n\tglobal_load_lds_dwordx4 %0, %4\n\tglobal_load_lds_dwordx4 %1, %4 offset:1024\n\tglobal_load_lds_dwordx4 %2, %4 offset:2048\n\tglobal_load_lds_dwordx4 %3, %4 offset:3072"
                 ::"v"(v[0]), "v"(v[1]), "v"(v[2]), "v"(v[3]), "s"(sbase), "s"(lds_dst) : "memory");
}
template <int OFF> DI void gload16_sc1(bf16x8& dst, unsigned voff, const char* sbase) {
    asm volatile("global_load_dwordx4 %0, %1, %2 offset:%3 sc1" : "=v"(dst) : "v"(voff), "s"(sbase), "i"(OFF) : "memory");
}
DI void gload4_sc1(unsigned& dst, unsigned voff, const void* sbase) {
    asm volatile("global_load_dword %0, %1, %2 sc1" : "=v"(dst) : "v"(voff), "s"(sbase) : "memory");
}
template <int N> DI void wait_vm_dep(bf16x8& v) { asm volatile("s_waitcnt vmcnt(%1)" : "+v"(v) : "i"(N)); }
template <int N> DI void wait_vm_dep2(bf16x8& v, bf16x8& w) { asm volatile("s_waitcnt vmcnt(%2)" : "+v"(v), "+v"(w) : "i"(N)); }
template <int N> DI void wait_vm_u(unsigned& v) { asm volatile("s_waitcnt vmcnt(%1)" : "+v"(v) : "i"(N)); }
template <int N> DI void wait_vm() { asm volatile("s_waitcnt vmcnt(%0)" ::"i"(N) : "memory"); }
#define WAIT_LGKM0() asm volatile("s_waitcnt lgkmcnt(0)" ::: "memory")
#define BAR() do { asm volatile("s_waitcnt lgkmcnt(0)" ::: "memory"); __builtin_amdgcn_s_barrier(); asm volatile("" ::: "memory"); } while (0)
#define BBAR() do { asm volatile("" ::: "memory"); __builtin_amdgcn_s_barrier(); asm volatile("" ::: "memory"); } while (0)
#define SB() __builtin_amdgcn_sched_barrier(0)
template <int... Is, class Fn> DI void sfor_impl(std::integer_sequence<int, Is...>, Fn&& f) { (f(std::integral_constant<int, Is>{}), ...); }
template <int N, class Fn> DI void sfor(Fn&& f) { sfor_impl(std::make_integer_sequence<int, N>{}, f); }
template <int V> using IC = std::integral_constant<int, V>;
DI void pin(bf16x8& v) { asm volatile("" : "+v"(v)); }
DI float glu_a(float g) { asm volatile("v_mul_f32 %0, 0xbfb8aa3b, %0\n\tv_exp_f32 %0, %0" : "+v"(g)); return g; }
DI float glu_b(float e, float u) { asm volatile("v_add_f32 %0, 1.0, %0\n\tv_rcp_f32 %0, %0\n\ts_nop 0\n\tv_mul_f32 %0, %0, %1" : "+v"(e) : "v"(u)); return e; }
template <int X, int ADD> DI unsigned xor_add(unsigned base) {
    unsigned r; asm volatile("v_xor_b32 %0, %2, %1\n\tv_add_u32 %0, %3, %0" : "=&v"(r) : "v"(base), "i"(X), "i"(ADD)); return r;
}
DI unsigned opaque(unsigned v) { asm volatile("" : "+v"(v)); return v; }
DI int fresh_lane() { int l; asm volatile("v_mbcnt_lo_u32_b32 %0, -1, 0\n\tv_mbcnt_hi_u32_b32 %0, -1, %0" : "=v"(l)); return l; }
DI bf16x8 tr_frag_o(unsigned base, int off0, int off1) {
    s16x4 lo = __builtin_amdgcn_ds_read_tr16_b64_v4i16((LAS s16x4*)((LAS char*)(size_t)base + off0));
    s16x4 hi = __builtin_amdgcn_ds_read_tr16_b64_v4i16((LAS s16x4*)((LAS char*)(size_t)base + off1));
    s16x8 f = __builtin_shufflevector(lo, hi, 0, 1, 2, 3, 4, 5, 6, 7);
    return __builtin_bit_cast(bf16x8, f);
}
DI u32x4 lds_rd128(unsigned a) { return *(LAS u32x4*)(size_t)a; }
DI unsigned lds_rd32(unsigned a) { return *(LAS unsigned*)(size_t)a; }
#define MFMA32(a, b, c) __builtin_amdgcn_mfma_f32_32x32x16_bf16((a), (b), (c), 0, 0, 0)
#define MFMA16(a, b, c) __builtin_amdgcn_mfma_f32_16x16x32_bf16((a), (b), (c), 0, 0, 0)

typedef __amdgpu_buffer_rsrc_t rsrc_t;
DI void st16_sc1(rsrc_t r, unsigned off, u32x4 v) { __builtin_amdgcn_raw_buffer_store_b128(v, r, off, 0, 16); }
DI void st16_sc1s(rsrc_t r, unsigned voff, unsigned soff, u32x4 v) {
    asm volatile("buffer_store_dwordx4 %0, %1, %2, %3 offen sc1\n\ts_nop 1" ::"v"(v), "v"(voff), "s"(r), "s"(soff) : "memory");
}
DI void st4_sc1(rsrc_t r, unsigned off, unsigned v) { __builtin_amdgcn_raw_buffer_store_b32(v, r, off, 0, 16); }
DI void flag_store(unsigned* sy, int idx, unsigned tag) { __hip_atomic_store((GAS unsigned*)sy + idx, tag, __ATOMIC_RELAXED, __HIP_MEMORY_SCOPE_AGENT); }
DI unsigned flag_load(const unsigned* sy, int idx) { return __hip_atomic_load((GAS unsigned*)sy + idx, __ATOMIC_RELAXED, __HIP_MEMORY_SCOPE_AGENT); }
constexpr unsigned kSpinCap = 1u << 20;
DI void wait_flags(const unsigned* sy, int idx, unsigned tag) {
    for (unsigned spins = 0; spins < kSpinCap; ++spins) {
        const unsigned v = flag_load(sy, idx);
        if (__all(v == tag)) break;
        __builtin_amdgcn_s_sleep(1);
    }
    asm volatile("" ::: "memory");
}

#define STAMP(i) do {} while (0)

template <int CTRL, int RMASK> DI float dpp_add(float v) {
    return v + __int_as_float(__builtin_amdgcn_update_dpp(0, __float_as_int(v), CTRL, RMASK, 0xf, true));
}
DI float wave_sum(float v) {
    v = dpp_add<0x111, 0xf>(v); v = dpp_add<0x112, 0xf>(v); v = dpp_add<0x114, 0xf>(v); v = dpp_add<0x118, 0xf>(v);
    v = dpp_add<0x142, 0xa>(v); v = dpp_add<0x143, 0xc>(v);
    return __int_as_float(__builtin_amdgcn_readlane(__float_as_int(v), 63));
}

constexpr int SLOT = 24576;
constexpr int SPARE = SLOT * 6;
constexpr int LDS_BYTES = 163840;
constexpr int KPOS = 4 * SLOT, VPOS = 5 * SLOT;
constexpr int WSL = 27648;
constexpr int WQ0 = 0, WQ1 = WSL, WQ2 = 2 * WSL, WQ3 = 3 * WSL, WQ4 = 4 * WSL, OTOP = 5 * WSL;
static_assert(WQ3 <= KPOS && OTOP + 18 * 1024 <= LDS_BYTES, "lds");

struct Ctx {
    const char* wq; const char* wgu;
    unsigned vq0, vq1, vq2, vg0, vg1, vg2;
    unsigned ldsbase;
    int wave; int P0;
};
DI void issue_q(const Ctx& c, int chunk, int pos) { glds16x3(c.wq + (size_t)(chunk * 192) * 3456, c.vq0, c.vq1, c.vq2, c.ldsbase + pos * SLOT + c.wave * 3072); }
DI void issue_gu(const Ctx& c, int panel, int chunk, int pos) {
    glds16x3(c.wgu + (size_t)(chunk * 96) * 9216 + (size_t)(c.P0 + panel) * 128, c.vg0, c.vg1, c.vg2, c.ldsbase + pos * SLOT + c.wave * 3072);
}

constexpr int PF1 = 3;
DI void issue_slot(const Ctx& c, int m) {
    if (m < 6) issue_gu(c, 1, m, m % 6);
    else if (m < 9) issue_q(c, m - 6, m % 6);
    else if (m < 15) issue_gu(c, 0, m - 9, m % 6);
    else issue_gu(c, 2, m - 15, m % 6);
}

struct Hand {
    unsigned* sy; unsigned tag; rsrc_t rs; int tt, j, c4, cgrp;
    const char* kb; const char* vb; const char* qb;
};

constexpr int NSTEPS = 396;
struct Step { short slot, ks, t, set, tau, first, fmt; };
struct Sched {
    Step st[NSTEPS];
    short fill[NSTEPS + 100][3];
    short nrows;
    short qn;
    short qent[24][5];
    short qlast;
};
template <int C4> constexpr Sched make_sched() {
    Sched S{};
    const int NTa = 4 - C4, NTc = 1 + C4;
    int setsA[4] = {2, 3, 0, 1};
    int setsC[4] = {0, 0, 0, 0};
    {
        int k = 0;
        for (int s = 0; s < 4; ++s) { int order[4] = {3, 0, 1, 2}; int cand = order[s]; bool used = false; for (int t = 0; t < NTa; ++t) used |= (setsA[t] == cand); if (!used && k < NTc - 1) setsC[k++] = cand; }
        setsC[NTc - 1] = setsA[0];
    }
    int n = 0, lastStep[11] = {};
    for (int slot = 0; slot < 21; ++slot) {
        int pass = slot < 6 ? 0 : slot < 9 ? 1 : slot < 15 ? 2 : 3;
        int i = pass == 0 ? slot : pass == 1 ? slot - 6 : pass == 2 ? slot - 9 : slot - 15;
        int nt = pass == 0 ? 4 : pass == 1 ? 2 : pass == 2 ? NTa : NTc;
        int kps = pass == 1 ? 12 : 6;
        int nsl = pass == 1 ? 3 : 6;
        int tau0 = pass == 0 ? 0 : pass == 1 ? 4 : pass == 2 ? 6 : 6 + NTa;
        bool tmajor = (i == nsl - 1) || (i == 0 && pass > 0);
        for (int k = 0; k < nt * kps; ++k) {
            int t = tmajor ? k / kps : k % nt, s = tmajor ? k % kps : k / nt;
            Step e{};
            e.slot = slot; e.ks = i * kps + s; e.t = t; e.tau = tau0 + t; e.fmt = pass == 1; e.first = (e.ks == 0);
            e.set = pass == 0 ? t : pass == 1 ? t : pass == 2 ? setsA[t] : setsC[t];
            S.st[n] = e; lastStep[e.tau] = n; ++n;
        }
    }
    const int delta = C4 == 0 ? 0 : C4 == 1 ? 6 : C4 == 2 ? 4 : 2;
    int qn = 0;
    for (int sl = 0; sl < 3; ++sl)
        for (int t = 0; t < 2; ++t)
            for (int r = 0; r < 16; r += 2) {
                int m = 0, d0 = 0;
                for (int hh = 1; hh >= 0; --hh) { int nn = 32 * t + (r & 3) + 8 * (r >> 2) + 4 * hh - delta; if (nn >= 0 && nn < 54 && nn / 18 == sl) { m |= 1 << hh; d0 = nn % 18 - 4 * hh; } }
                if (m) { S.qent[qn][0] = t; S.qent[qn][1] = r; S.qent[qn][2] = sl; S.qent[qn][3] = m; S.qent[qn][4] = d0; ++qn; }
            }
    S.qn = qn;
    int freeg = 0;
    auto put = [&](int g, int code) { for (int k = 0; k < 3; ++k) if (S.fill[g][k] == 0) { S.fill[g][k] = code; return; } S.fill[g][0] = -1; };
    for (int tau = 0; tau < 11; ++tau) {
        if (tau == 4) continue;
        int ready = lastStep[tau] + 3;
        int g = ready > freeg ? ready : freeg;
        int dl = 100000;
        for (int k = lastStep[tau] + 1; k < NSTEPS; ++k) if (S.st[k].first && (S.st[k].set == S.st[lastStep[tau]].set || (tau == 5 && S.st[k].set == S.st[lastStep[4]].set))) { dl = k; break; }
        int ops[40] = {}, cnt = 0;
        if (tau == 5) {
            for (int sl = 0; sl < 3; ++sl) {
                for (int e = 0; e < qn; ++e) if (S.qent[e][2] == sl) ops[cnt++] = 2000 + e;
                ops[cnt++] = 2100 + sl; ops[cnt++] = 2110 + sl;
            }
        } else {
            const int b = 1000 + tau * 32;
            ops[cnt++] = b + 0;
            for (int j = 0; j < 7; ++j) { ops[cnt++] = b + j + 1; ops[cnt++] = b + 8 + j; }
            ops[cnt++] = b + 15; ops[cnt++] = b + 16; ops[cnt++] = b + 17;
        }
        int avail = dl - g, dbl = cnt > avail ? cnt - avail : 0;
        if (tau == 5 && dbl < cnt / 2) dbl = cnt / 2;
        int k = 0;
        while (k < cnt) { put(g, ops[k++]); if (dbl > 0 && k < cnt) { put(g, ops[k++]); --dbl; } ++g; }
        if (tau == 5) { for (int q = 0; q < NSTEPS + 100; ++q) for (int z = 0; z < 3; ++z) if (S.fill[q][z] == 2112) S.qlast = q; }
        freeg = g;
    }
    S.nrows = freeg;
    return S;
}
template <int C4> struct SchedOf { static constexpr Sched SC = make_sched<C4>(); };
template <int C4> constexpr int set_of(int tau) { for (int g = 0; g < NSTEPS; ++g) if (SchedOf<C4>::SC.st[g].tau == tau) return SchedOf<C4>::SC.st[g].set; return 0; }
constexpr int xpos(int s) { return s < 6 ? -1 : 4 * ((s - 6) / 3) + 2; }
constexpr int dpos(int d) {
    return d == 2 ? -1 : d == 3 ? 4 * 3 + 1 : d == 4 ? 4 * 9 + 1 : d == 5 ? 4 * 15 + 1 : d == 6 ? 4 * 21 + 1 : d == 7 ? 4 * 27 + 1 : d == 8 ? 24 * 5 + 1
         : d == 9 || d == 10 ? 24 * 6 + 1 : d == 11 || d == 12 ? 24 * 7 + 1 : 24 * (d - 5) + 1;
}
constexpr bool d_after(int e, int d) { return dpos(e) > dpos(d) || (dpos(e) == dpos(d) && e > d); }
constexpr int younger_than_x(int s, int now) {
    int n = 0;
    for (int t = s + 1; t < 36; ++t) if (xpos(t) < now) ++n;
    for (int d = 2; d <= 20; ++d) if (dpos(d) < now && (dpos(d) > xpos(s) || (dpos(d) == xpos(s) && d == 2))) n += 3;
    return n;
}
constexpr int younger_than_d(int d, int now) {
    int n = 0;
    for (int t = 0; t < 36; ++t) if (xpos(t) < now && xpos(t) > dpos(d)) ++n;
    for (int e = 2; e <= 20; ++e) if (dpos(e) < now && d_after(e, d)) n += 3;
    return n;
}

template <int C4>
DI void gemm1(const Ctx& c, const Hand& hd, char* ws, int tile32) {
#define S (SchedOf<C4>::SC)
    constexpr int NTa = 4 - C4;
    constexpr int NK = (C4 <= 1) ? 17 : (C4 == 2 ? 18 : 19), NV = (C4 <= 2) ? 18 : 19;
    constexpr int NCHK = C4 == 0 ? 14 : C4 == 1 ? 15 : C4 == 2 ? 17 : 18;
    constexpr int NQF = S.st[S.qlast < NSTEPS ? S.qlast : NSTEPS - 1].slot + 2;
    static_assert(NQF <= 13 && NQF >= 9, "qkv publish");
    unsigned Lgu, Lq = 0, l16;
    {
        const int lane = fresh_lane();
        const int hh = lane >> 5, blk = (lane >> 4) & 1, q = (lane & 15) >> 2, pp = lane & 3;
        Lgu = c.ldsbase + (unsigned)(blk * 12288 + (8 * hh + q) * 128 + 8 * pp + 32 * (((q >> 1) & 1) | (blk << 1)));
        l16 = lane * 16;
    }
    const unsigned hbs = (unsigned)WS_H + ((unsigned)tile32 * 288u + (unsigned)c.P0 * 4u) * 1024u;
    const unsigned stg = c.ldsbase + SPARE + c.wave * 2048;
    const unsigned qds = (unsigned)WS_QKV + (unsigned)((hd.cgrp * 3) * 2048 + tile32 * 32) * 48u;
    unsigned stw = 0;
    const char* xs = ws + WS_X + (size_t)tile32 * 36 * 1024;
    bf16x8 xf[36];
    f32x16 acc[4];
    unsigned cur[4];
    bf16x8 A[PF1 + 1];
    unsigned qflag = 0, pl0 = 0, pl1 = 0;
    sfor<6>([&](auto SI) { constexpr int s = SI; gload16_sc1<(s & 3) * 1024>(xf[s], l16, xs + (s >> 2) * 4096); });
    issue_slot(c, 2);
    auto rd = [&](auto IDX) {
        constexpr Step e = S.st[IDX];
        constexpr int kl = e.fmt ? e.ks % 12 : e.ks % 6, pos = e.slot % 6;
        constexpr int col = e.fmt ? e.t : (e.slot >= 9 && e.slot < 15 ? C4 + e.t : e.t);
        if constexpr (e.fmt && e.ks == 0 && e.t == 0) {
            const int lane = fresh_lane();
            const int hh = lane >> 5, blk = (lane >> 4) & 1, q = (lane & 15) >> 2, pp = lane & 3;
            Lq = c.ldsbase + (unsigned)((8 * hh + q) * 128 + 8 * pp + 32 * (blk ^ (((q >> 1) & 1) << 1)));
        }
        if constexpr (kl == 0) cur[e.t] = e.fmt ? xor_add<(col << 6), pos * SLOT>(Lq) : xor_add<(col << 5), pos * SLOT>(Lgu);
        return tr_frag_o(cur[e.t], kl * 2048, kl * 2048 + 512);
    };
    auto filler = [&](auto CODE) {
        constexpr int code = CODE;
        if constexpr (code >= 1000 && code < 2000) {
            constexpr int tau = (code - 1000) / 32, j = (code - 1000) % 32;
            constexpr int realset = set_of<C4>(tau);
            constexpr int fgo = tau < 4 ? 4 + tau : (tau < 6 + NTa ? C4 + tau - 6 : 8 + tau - 6 - NTa);
            if constexpr (j < 8) acc[realset][j] = glu_a(acc[realset][j]);
            else if constexpr (j < 16) acc[realset][j - 8] = glu_b(acc[realset][j - 8], acc[realset][j]);
            else if constexpr (j == 16) {
                pl0 = pk2(acc[realset][0], acc[realset][1]);
                pl1 = pk2(acc[realset][2], acc[realset][3]);
            } else {
                u32x4 w = {pl0, pl1, pk2(acc[realset][4], acc[realset][5]), pk2(acc[realset][6], acc[realset][7])};
                st16_sc1s(hd.rs, l16, hbs + fgo * 1024, w);
            }
        } else if constexpr (code >= 2000 && code < 2100) {
            constexpr int e = code - 2000, t = S.qent[e][0], r = S.qent[e][1], m = S.qent[e][3], d0 = S.qent[e][4];
            if constexpr (e == 0) { const int ln = fresh_lane(); stw = opaque(stg + (ln & 31) * 48 + ((ln >> 5) << 3)); }
            const float qs = (hd.cgrp * 3 + S.qent[e][2] < 32) ? 0.23570226039551584f * 1.4426950408889634f : 1.f;
            const unsigned v = pk2(acc[t][r] * qs, acc[t][r + 1] * qs);
            if constexpr (m == 3) *(LAS unsigned*)(size_t)(stw + 2 * d0) = v;
            else if ((int)((stw >> 3) & 1) == (m >> 1)) *(LAS unsigned*)(size_t)(stw + 2 * d0) = v;
        } else if constexpr (code >= 2100 && code < 2110) {
            constexpr int sl = code - 2100;
            if (((stw >> 3) & 1) == 0) *(LAS unsigned*)(size_t)(stw + 36) = (hd.cgrp * 3 + sl >= 64) ? 0x00003F80u : 0u;
            else *(LAS u32x2*)(size_t)(stw + 32) = (u32x2){0u, 0u};
        } else if constexpr (code >= 2110) {
            constexpr int sl = code - 2110;
            const u32x4 w0 = lds_rd128(stg + l16);
            st16_sc1s(hd.rs, l16, qds + (unsigned)sl * 2048u * 48u, w0);
            if (fresh_lane() < 32) { const u32x4 w1 = lds_rd128(stg + 1024 + l16); st16_sc1s(hd.rs, l16, qds + (unsigned)sl * 2048u * 48u + 1024, w1); }
        }
    };
    sfor<NSTEPS>([&](auto IDX) {
        constexpr int idx = IDX;
        constexpr Step e = S.st[idx];
        constexpr bool top = (idx == 0) || (S.st[idx > 0 ? idx - 1 : 0].slot != e.slot);
        if constexpr (top) {
            constexpr int n = e.slot;
            if constexpr (n == 5) {
                wait_vm<(younger_than_x(35, 120) < younger_than_d(6, 120) ? younger_than_x(35, 120) : younger_than_d(6, 120))>();
                pin(xf[30]); pin(xf[31]); pin(xf[32]); pin(xf[33]); pin(xf[34]); pin(xf[35]);
            } else if constexpr (n == NQF) wait_vm<3>();
            else if constexpr (n >= 1 && n <= 16) wait_vm<younger_than_d(n + 1, 24 * n)>();
            else if constexpr (n == 17) wait_vm<6>();
            else if constexpr (n == 18) wait_vm<3 + (NK <= 17 ? 3 : 0)>();
            else if constexpr (n == 19) wait_vm<(NK <= 18 ? 3 : 0) + (NV <= 18 ? 5 : 0)>();
            if constexpr (n == NCHK) {
                if (c.wave == 0) {
                    wait_vm_u<(n + 4 <= 20 ? 3 : 0)>(qflag);
                    if (!__all(qflag == hd.tag)) { const int ln = fresh_lane(); wait_flags(hd.sy, SY_QF + ((ln >= 32 && hd.tt > 0) ? hd.tt - 1 : hd.tt) * 64 + (ln & 31), hd.tag); }
                    asm volatile("buffer_inv sc1" ::: "memory");
                }
            }
            if constexpr (n == NK) { if (c.wave == 0) wait_vm<(NCHK + 5 <= 20 ? 3 * (20 - (NCHK + 5) + 1) : 0)>(); }
            if constexpr (n > 0) BBAR();
            if constexpr (n == NQF) { if (c.wave == 0 && fresh_lane() == 0) flag_store(hd.sy, SY_QF + hd.tt * 64 + hd.cgrp, hd.tag); }
            if constexpr (n == NCHK - 1) { if (c.wave == 0) { const int lane = fresh_lane(); gload4_sc1(qflag, (unsigned)(SY_QF + ((lane >= 32 && hd.tt > 0) ? hd.tt - 1 : hd.tt) * 64 + (lane & 31)) * 4u, hd.sy); } }
            sfor<19>([&](auto DD) { constexpr int d = DD + 2; if constexpr (dpos(d) == 24 * n + 1 && n >= 5) issue_slot(c, d); });
            if constexpr (n == NK || n == NV) {
#pragma unroll
                for (int kv = 0; kv < 2; ++kv) {
                    if ((kv == 0 && n == NK) || (kv == 1 && n == NV)) {
#pragma unroll
                        for (int k = 0; k < 3; ++k) {
                            const int m = c.wave * 3 + k;
                            int off = (hd.tt * 256 - 256) * 48 + m * 1024;
                            if (off < 0) off = m * 1024;
                            glds16((kv == 0 ? hd.kb : hd.vb) + off, l16, c.ldsbase + (kv == 0 ? KPOS : VPOS) + m * 1024);
                        }
                    }
                }
            }
            if constexpr (n == NV) {
#pragma unroll
                for (int k = 0; k < 2; ++k) {
                    int m = c.wave * 2 + k; if (m > 11) m = 11;
                    glds16(hd.qb + m * 1024, l16, c.ldsbase + SPARE + m * 1024);
                }
            }
            if constexpr (idx == 0) sfor<PF1>([&](auto J) { A[J] = rd(J); });
        }
        if constexpr (idx + PF1 < NSTEPS) A[(idx + PF1) % (PF1 + 1)] = rd(IC<idx + PF1>{});
        if constexpr (e.slot < 5 && e.t == 0) {
            sfor<6>([&](auto DD) { constexpr int d = DD + 3; if constexpr (d <= 7 && dpos(d) == 4 * e.ks + 1) issue_slot(c, d); });
            sfor<3>([&](auto JJ) { constexpr int sx = 6 + 3 * e.ks + JJ; if constexpr (sx < 36) gload16_sc1<(sx & 3) * 1024>(xf[sx], l16, xs + (sx >> 2) * 4096); });
            wait_vm_dep<younger_than_x(e.ks, 4 * e.ks + 3)>(xf[e.ks]);
        }
        SB();
        if constexpr (e.first) {
            f32x16 z;
#pragma unroll
            for (int r = 0; r < 16; ++r) z[r] = 0.f;
            acc[e.set] = MFMA32(A[idx % (PF1 + 1)], xf[e.ks], z);
        } else acc[e.set] = MFMA32(A[idx % (PF1 + 1)], xf[e.ks], acc[e.set]);
        SB();
        if constexpr (S.fill[idx][0] != 0) {
            filler(IC<S.fill[idx][0]>{});
            if constexpr (S.fill[idx][1] != 0) filler(IC<S.fill[idx][1]>{});
            if constexpr (S.fill[idx][2] != 0) filler(IC<S.fill[idx][2]>{});
            SB();
        }
    });
    sfor<S.nrows - NSTEPS>([&](auto R) {
        constexpr int g = NSTEPS + R;
        if constexpr (S.fill[g][0] != 0) filler(IC<S.fill[g][0]>{});
        if constexpr (S.fill[g][1] != 0) filler(IC<S.fill[g][1]>{});
        if constexpr (S.fill[g][2] != 0) filler(IC<S.fill[g][2]>{});
    });
}
#undef S

constexpr int g2_ks(int u) { constexpr int o[18] = {0, 1, 2, 3, 5, 6, 7, 9, 10, 11, 14, 15, 4, 8, 12, 13, 16, 17}; return o[u]; }
constexpr int g2_place(int sl) { return sl == 0 ? WQ0 : sl == 1 ? WQ1 : sl == 2 ? WQ2 : sl == 3 ? WQ3 : sl == 4 ? WQ4 : WQ0; }
constexpr int ewait(int u) { constexpr int c[8] = {0, 4, 8, 16, 16, 20, 24, 24}; return 2 * (11 - u) + 2 + c[u]; }

__global__ __launch_bounds__(512, 2) void mega_kernel(const u16* __restrict__ x, const u16* __restrict__ wqkv, const u16* __restrict__ wo,
                                                      const u16* __restrict__ wg, const u16* __restrict__ wu, const u16* __restrict__ wd,
                                                      char* __restrict__ ws, u16* __restrict__ out) {
    extern __shared__ __attribute__((aligned(16))) char smem[];
    const int tid = threadIdx.x;
    const int wave = __builtin_amdgcn_readfirstlane(tid >> 6);
    const int bid = blockIdx.x, xj = bid & 7, loc = bid >> 3;
    const int c4 = loc & 3, tt = loc >> 2, cgrp = xj * 4 + c4;
    const unsigned ldsbase = (unsigned)(size_t)(LAS char*)smem;
    unsigned* sy = (unsigned*)(ws + WS_SYNC);
    const rsrc_t rs = __builtin_amdgcn_make_buffer_rsrc(ws, 0, (unsigned)WS_USED, 0x00020000);
    unsigned tag;
    Ctx c;

    STAMP(0);
    {
        const int lane = tid & 63;
        const int xrow = bid * 8 + wave;
        const char* xr = (const char*)x + (size_t)xrow * 1152;
        u32x4 xv0 = *(const u32x4*)(xr + lane * 16);
        u32x4 xv1 = *(const u32x4*)(xr + 1024 + (lane & 7) * 16);
        const unsigned gen = flag_load(sy, SY_GEN);
        const int qa0 = (108 * cgrp) & ~15;
        c.wq = (const char*)wqkv + qa0; c.wgu = (const char*)((wave >> 2) ? wu : wg);
        c.ldsbase = ldsbase; c.wave = wave; c.P0 = (9 * cgrp) >> 2;
        {
            const int lrow = lane >> 3, c16 = lane & 7, bsel = wave >> 2;
            const int swgu = ((lrow >> 1) & 1) | (bsel << 1);
            const int chgu = (((c16 >> 1) ^ swgu) << 1) | (c16 & 1);
            c.vg0 = (unsigned)((24 * (wave & 3) + lrow) * 9216 + chgu * 16); c.vg1 = c.vg0 + 8 * 9216 - 1024; c.vg2 = c.vg0 + 16 * 9216 - 2048;
            const int swq = ((lrow >> 1) & 1) << 1;
            int chq = (((c16 >> 1) ^ swq) << 1) | (c16 & 1);
            if (cgrp == 31 && chq == 7) chq = 6;
            c.vq0 = (unsigned)((24 * wave + lrow) * 3456 + chq * 16); c.vq1 = c.vq0 + 8 * 3456 - 1024; c.vq2 = c.vq0 + 16 * 3456 - 2048;
        }
        tag = (unsigned)__builtin_amdgcn_readfirstlane((int)(gen * 0x9E3779B1u + 0x7F4A7C15u));
        float s1 = 0.f;
        if (lane >= 8) xv1 = (u32x4){0u, 0u, 0u, 0u};
#pragma unroll
        for (int k = 0; k < 4; ++k) { s1 = dot2(xv0[k], 0x3f803f80u, s1); s1 = dot2(xv1[k], 0x3f803f80u, s1); }
        const float meanf = wave_sum(s1) * (1.f / 576.f);
        float s2 = 0.f;
#pragma unroll
        for (int k = 0; k < 4; ++k) {
            float a = lo2f(xv0[k]) - meanf, b = hi2f(xv0[k]) - meanf; s2 += a * a + b * b;
            if (lane < 8) { a = lo2f(xv1[k]) - meanf; b = hi2f(xv1[k]) - meanf; s2 += a * a + b * b; }
        }
        const float var = rbf(wave_sum(s2) * (1.f / 576.f));
        const float mean = rbf(meanf);
        const float rden = 1.f / rbf(sqrtf(rbf(var + 1e-5f)));
        u32x4 o0, o1;
#pragma unroll
        for (int k = 0; k < 4; ++k) {
            o0[k] = pk2(rbf(lo2f(xv0[k]) - mean) * rden, rbf(hi2f(xv0[k]) - mean) * rden);
            o1[k] = pk2(rbf(lo2f(xv1[k]) - mean) * rden, rbf(hi2f(xv1[k]) - mean) * rden);
        }
        *(LAS u32x4*)(size_t)(ldsbase + SPARE + (lane * 8 + wave) * 16) = o0;
        if (lane < 8) *(LAS u32x4*)(size_t)(ldsbase + SPARE + ((64 + lane) * 8 + wave) * 16) = o1;
        BAR();
        const unsigned xd = (unsigned)WS_X + (unsigned)(bid >> 2) * 36u * 1024u;
        const int t0 = (bid & 3) * 8;
        {
            const int i = tid, cc = i >> 3, tk = i & 7;
            st16_sc1(rs, xd + ((cc >> 1) * 64 + (cc & 1) * 32 + t0 + tk) * 16, lds_rd128(ldsbase + SPARE + i * 16));
        }
        {
            const int i = 512 + lane, cc = i >> 3, tk = i & 7;
            st16_sc1(rs, wave == 0 ? xd + ((cc >> 1) * 64 + (cc & 1) * 32 + t0 + tk) * 16 : 0xFFFFFF00u, lds_rd128(ldsbase + SPARE + i * 16));
        }
        issue_slot(c, 0); issue_slot(c, 1);
        wait_vm<6>();
        BBAR();
        if (tid == 0) { flag_store(sy, SY_XF + (bid >> 5) * 64 + (bid & 31), tag); flag_store(sy, SY_START + bid, tag); }
    }
    STAMP(1);
    Hand hd;
    hd.sy = sy; hd.tag = tag; hd.rs = rs; hd.tt = tt; hd.j = xj; hd.c4 = c4; hd.cgrp = cgrp;
    hd.kb = ws + WS_QKV + ((size_t)(32 + cgrp) * 2048) * 48; hd.vb = ws + WS_QKV + ((size_t)(64 + cgrp) * 2048) * 48;
    hd.qb = ws + WS_QKV + ((size_t)cgrp * 2048 + tt * 256) * 48;

    if (wave == 0) wait_flags(sy, SY_XF + tt * 64 + (fresh_lane() & 31), tag);
    wait_vm<0>();
    BBAR();
    STAMP(2);
    const int tile32 = tt * 8 + wave;
    if (c4 == 0) gemm1<0>(c, hd, ws, tile32);
    else if (c4 == 1) gemm1<1>(c, hd, ws, tile32);
    else if (c4 == 2) gemm1<2>(c, hd, ws, tile32);
    else gemm1<3>(c, hd, ws, tile32);
    STAMP(7);

    {
    const int lane = fresh_lane();
    const int hh = lane >> 5, blk = (lane >> 4) & 1, q = (lane & 15) >> 2, pp = lane & 3;
    const unsigned l16 = lane * 16;
    const bool tid0 = (wave == 0) && (lane == 0);
    const char* wdb = (const char*)wd + (size_t)(xj * 576) * 1152 + c4 * 288;
    const int mb = wave < 6 ? 4 * wave : 23;
    unsigned vo[4];
#pragma unroll
    for (int i = 0; i < 4; ++i) {
        const int n = (mb + i) * 64 + lane, row = n / 18, cc = n - row * 18;
        vo[i] = (unsigned)(row * 1152 + cc * 16 - 1024 * i);
    }
    auto issue_wd = [&](int slot, int dst) { glds16x4(wdb + (size_t)slot * 96 * 1152, vo, ldsbase + dst + mb * 1024); };
    const int q4 = lane >> 4, t16 = lane & 15, qq = (lane & 15) >> 2;
    const char* hsrc = ws + WS_H + ((size_t)tile32 * 288 + xj * 36) * 1024;
    const unsigned hvo = (unsigned)(((q4 >> 1) * 64 + (q4 & 1) * 32 + t16) * 16);
    bf16x8 hf[18][2];
    auto hload = [&](auto UI) { constexpr int u = UI, s = g2_ks(u);
        gload16_sc1<(s & 1) * 2048>(hf[u][0], hvo, hsrc + (s >> 1) * 4096);
        gload16_sc1<(s & 1) * 2048 + 256>(hf[u][1], hvo, hsrc + (s >> 1) * 4096); };
    unsigned hflag = 0;
    const int hfi = (xj * 8 + tt) * 64;
    {
        if (c4 == 0) wait_vm<1>(); else if (c4 == 1) wait_vm<2>(); else if (c4 == 2) wait_vm<3>(); else wait_vm<4>();
        BBAR();
        if (tid0) flag_store(sy, SY_HF + hfi + c4, tag);
        issue_wd(0, WQ0); issue_wd(1, WQ1); issue_wd(2, WQ2);
        STAMP(8);
        const int t = lane & 31;
        const int q0 = tt * 256 + wave * 32;
        bf16x8 Qf0, Qf1;
        {
            const unsigned qa = ldsbase + SPARE + (wave * 32 + t) * 48;
            Qf0 = __builtin_bit_cast(bf16x8, lds_rd128(qa + 16 * hh));
            const unsigned qv1 = lds_rd32(qa + 32);
            u32x4 q1 = {hh == 0 ? qv1 : 0u, 0u, 0u, 0u};
            Qf1 = __builtin_bit_cast(bf16x8, q1);
        }
        const unsigned kaddr = ldsbase + KPOS + (wave * 32 + t) * 48;
        const unsigned vaddr = ldsbase + VPOS + (wave * 32 + 4 * hh + q) * 48 + 32 * blk + 8 * pp;
        const float NEG = -__builtin_inff();
        f32x16 o;
        auto attend = [&](auto FULLC) {
            constexpr bool FULL = FULLC;
            const int ktmin = FULL ? 0 : 8 - wave;
            f32x16 sc[9];
#pragma unroll
            for (int kt = 0; kt < 9; ++kt) {
                if (FULL || kt >= ktmin) {
                    u32x4 k0 = lds_rd128(kaddr + kt * 1536 + 16 * hh);
                    u32x4 k1 = {lds_rd32(kaddr + kt * 1536 + 32), 0u, 0u, 0u};
                    f32x16 z;
#pragma unroll
                    for (int r = 0; r < 16; ++r) z[r] = 0.f;
                    z = MFMA32(__builtin_bit_cast(bf16x8, k0), Qf0, z);
                    sc[kt] = MFMA32(__builtin_bit_cast(bf16x8, k1), Qf1, z);
                } else {
#pragma unroll
                    for (int r = 0; r < 16; ++r) sc[kt][r] = NEG;
                }
            }
#pragma unroll
            for (int r = 0; r < 16; ++r) {
                const int kk = (r & 3) + 8 * (r >> 2) + 4 * hh;
                if (!(kk > t)) sc[0][r] = NEG;
                if (!(kk <= t)) sc[8][r] = NEG;
            }
            float m = NEG;
#pragma unroll
            for (int kt = 0; kt < 9; ++kt)
#pragma unroll
                for (int r = 0; r < 16; ++r) m = fmaxf(m, sc[kt][r]);
            m = xhalf_max(m);
            if (wave == 0) gload4_sc1(hflag, (unsigned)(SY_HF + hfi + (lane & 3)) * 4u, sy);
#pragma unroll
            for (int r = 0; r < 16; ++r) o[r] = 0.f;
            sfor<9>([&](auto KT) {
                constexpr int kt = KT;
                if constexpr (kt == 2) {
                    wait_vm<12>();
                    if (wave == 0) {
                        wait_vm_u<0>(hflag);
                        if (!__all(hflag == tag)) wait_flags(sy, SY_HF + hfi + (lane & 3), tag);
                    }
                    BBAR();
                    if (tid0) flag_store(sy, SY_H2 + hfi + c4, tag);
                }
                if constexpr (kt == 6) { if (wave == 0) gload4_sc1(hflag, (unsigned)(SY_H2 + hfi + (lane & 3)) * 4u, sy); }
                if (FULL || kt >= ktmin) {
                    unsigned pk[8];
#pragma unroll
                    for (int r = 0; r < 16; r += 2)
                        pk[r >> 1] = pk2(__builtin_amdgcn_exp2f(sc[kt][r] - m), __builtin_amdgcn_exp2f(sc[kt][r + 1] - m));
#pragma unroll
                    for (int s = 0; s < 2; ++s) {
                        u32x4 pb = {pk[4 * s], pk[4 * s + 1], pk[4 * s + 2], pk[4 * s + 3]};
                        bf16x8 A = tr_frag_o(vaddr, (kt * 32 + 16 * s) * 48, (kt * 32 + 16 * s + 8) * 48);
                        o = MFMA32(A, __builtin_bit_cast(bf16x8, pb), o);
                    }
                }
                if constexpr (kt >= 3) { SB(); hload(IC<2 * (kt - 3)>{}); hload(IC<2 * (kt - 3) + 1>{}); SB(); }
            });
        };
        if (tt > 0) attend(std::true_type{}); else attend(std::false_type{});
        const float inv = 1.f / lo_bcast(o[10]);
        const int tile16 = (q0 >> 4) + ((lane >> 4) & 1);
        const unsigned ob = (unsigned)WS_O + (unsigned)(tile16 * 18) * 1024u + t16 * 16;
        u32x4 w = {pk2(o[0] * inv, o[1] * inv), pk2(o[2] * inv, o[3] * inv), pk2(o[4] * inv, o[5] * inv), pk2(o[6] * inv, o[7] * inv)};
        st16_sc1(rs, ob + (cgrp >> 1) * 1024 + (2 * (cgrp & 1) + hh) * 256, w);
        st4_sc1(rs, hh == 0 ? ob + (16 + (cgrp >> 4)) * 1024 + ((cgrp >> 2) & 3) * 256 + (cgrp & 3) * 4 : 0xFFFFFFF0u, pk2(o[8] * inv, o[9] * inv));
        if (wave == 0) {
            wait_vm_u<14>(hflag);
            if (!__all(hflag == tag)) wait_flags(sy, SY_H2 + hfi + (lane & 3), tag);
        }
    }
    STAMP(9);
    f32x4 acc[2][9];
    const unsigned arel = ldsbase + (unsigned)((16 * (q4 >> 1) + 4 * (q4 & 1) + qq) * 288 + 8 * pp);
    const unsigned pdu = (unsigned)WS_P + (unsigned)(((tt * 4 + c4) * 8 + wave) * 8 + xj) * 9u * 1024u;
    const char* wob = (const char*)wo + c4 * 288;
    unsigned vw[4];
    auto wo_offsets = [&](bool last) {
        const int ln = fresh_lane();
#pragma unroll
        for (int i = 0; i < 4; ++i) {
            const int n = (mb + i) * 64 + ln, rs_ = n / 18, cc = n - rs_ * 18;
            const int u1 = rs_ >> 5, r1 = rs_ & 31, qx = 2 * (r1 >> 4) + ((r1 >> 2) & 1), tx = (r1 >> 3) & 1, qy = r1 & 3;
            const int f = 18 * (qx >> 1) + 4 * (qx & 1) + 8 * tx + qy;
            const int jx = 4 * tx + qy, g = 4 * (u1 - 1) + qx;
            const int row5 = (u1 == 0) ? 36 * 15 + f : 18 * (4 * g + (jx >> 1)) + 16 + (jx & 1);
            vw[i] = (unsigned)((last ? row5 : 36 * u1 + f) * 1152 + cc * 16 - 1024 * i);
        }
    };
    auto issue_wo = [&](int slot, int dst) { glds16x4(slot < 5 ? wob + (size_t)slot * 108 * 1152 : wob, vw, ldsbase + dst + mb * 1024); };
    {
        wait_vm<26>();
        BAR();
        constexpr int PF = 4;
        bf16x8 A[PF + 1];
        unsigned oflag = 0;
        constexpr int NS0 = 126, NST = 162;
        auto st_u = [](int idx) constexpr { return idx < NS0 ? idx / 9 : 14 + (idx - NS0) % 4; };
        auto st_dt = [](int idx) constexpr { return idx < NS0 ? idx % 9 : (idx - NS0) / 4; };
        unsigned cur = 0, curl = 0;
        auto rd = [&](auto IDX) {
            constexpr int idx = IDX, u = st_u(idx), dt = st_dt(idx);
            constexpr int ks = g2_ks(u), off = g2_place(ks / 3) + (ks % 3) * 32 * 288;
            if constexpr (idx < NS0) {
                if constexpr (dt == 0) cur = xor_add<0, off>(arel);
                return tr_frag_o(cur, dt * 32, dt * 32 + 8 * 288);
            } else {
                if constexpr (idx == NS0) { cur = xor_add<0, 0>(arel); curl = xor_add<0, WQ4>(arel); }
                if constexpr (off >= WQ4) return tr_frag_o(curl, off - WQ4 + dt * 32, off - WQ4 + dt * 32 + 8 * 288);
                else return tr_frag_o(cur, off + dt * 32, off + dt * 32 + 8 * 288);
            }
        };
        sfor<NST>([&](auto IDX) {
            constexpr int idx = IDX, u = st_u(idx), dt = st_dt(idx);
            if constexpr (idx < NS0 && dt == 0) {
                if constexpr (u == 0) STAMP(10);
                if constexpr (u == 3) { BBAR(); issue_wd(5, WQ0); }
                if constexpr (u == 6) {
                    wait_vm<12>();
                    sfor<6>([&](auto K) { pin(hf[6 + K][0]); pin(hf[6 + K][1]); });
                    BBAR();
                    if (tid0) flag_store(sy, SY_OF + tt * 64 + cgrp, tag);
                }
                if constexpr (u == 8) { if (wave == 0) gload4_sc1(oflag, (unsigned)(SY_OF + tt * 64 + (lane & 31)) * 4u, sy); }
                if constexpr (u == 10) {
                    wait_vm<8>();
                    if (wave == 0) {
                        wait_vm_u<0>(oflag);
                        if (!__all(oflag == tag)) wait_flags(sy, SY_OF + tt * 64 + (lane & 31), tag);
                        asm volatile("buffer_inv sc1" ::: "memory");
                    }
                    BBAR();
                }
                if constexpr (u == 12) {
                    if (wave == 0) wait_vm<1>(); else wait_vm<0>();
                    sfor<6>([&](auto K) { pin(hf[12 + K][0]); pin(hf[12 + K][1]); });
                    wo_offsets(false);
                }
                if constexpr (idx == 0) sfor<PF>([&](auto J) { A[J] = rd(J); });
                if constexpr (u < 6) wait_vm_dep2<ewait(u)>(hf[u][0], hf[u][1]);
            }
            if constexpr (idx == NS0) {
                if (wave == 0) wait_vm<0>();
                BBAR();
                const char* osrc = ws + WS_O + (size_t)((tt * 8 + xj) * 2) * 18 * 1024;
#pragma unroll
                for (int i = 0; i < 5; ++i) {
                    int m = wave + 8 * i; if (m >= 36) m -= 8;
                    glds16(osrc + m * 1024, l16, ldsbase + (m < 18 ? OTOP + m * 1024 : WQ3 + (m - 18) * 1024));
                }
                issue_wo(0, WQ1); issue_wo(1, WQ2);
            }
            if constexpr (idx + PF < NST) A[(idx + PF) % (PF + 1)] = rd(IC<idx + PF>{});
            SB();
            if constexpr (u == 0) {
                acc[0][dt] = MFMA16(A[idx % (PF + 1)], hf[u][0], ((f32x4){0.f, 0.f, 0.f, 0.f}));
                acc[1][dt] = MFMA16(A[idx % (PF + 1)], hf[u][1], ((f32x4){0.f, 0.f, 0.f, 0.f}));
            } else {
                acc[0][dt] = MFMA16(A[idx % (PF + 1)], hf[u][0], acc[0][dt]);
                acc[1][dt] = MFMA16(A[idx % (PF + 1)], hf[u][1], acc[1][dt]);
            }
            SB();
            if constexpr (idx < NS0 && dt == 2) {
                if constexpr (u == 0) issue_wd(3, WQ3);
                if constexpr (u == 1) { hload(IC<12>{}); hload(IC<13>{}); }
                if constexpr (u == 2) issue_wd(4, WQ4);
                if constexpr (u == 4) { hload(IC<14>{}); hload(IC<15>{}); }
                if constexpr (u == 5) { hload(IC<16>{}); hload(IC<17>{}); }
                SB();
            }
            if constexpr (idx >= NS0 && u == 16 && dt > 0) {
                constexpr int d = dt - 1;
                u32x4 w = {pk2(acc[0][d][0], acc[0][d][1]), pk2(acc[0][d][2], acc[0][d][3]), pk2(acc[1][d][0], acc[1][d][1]), pk2(acc[1][d][2], acc[1][d][3])};
                st16_sc1s(rs, l16, pdu + d * 1024, w);
                SB();
            }
        });
        STAMP(16);
        u32x4 w = {pk2(acc[0][8][0], acc[0][8][1]), pk2(acc[0][8][2], acc[0][8][3]), pk2(acc[1][8][0], acc[1][8][1]), pk2(acc[1][8][2], acc[1][8][3])};
        st16_sc1s(rs, l16, pdu + 8 * 1024, w);
    }
    {
        wait_vm<0>();
        BAR();
        if (tid0) flag_store(sy, SY_PF + (tt * 4 + c4) * 64 + xj, tag);
        STAMP(17);
        issue_wo(2, WQ0); issue_wo(3, WQ4);
        const int tok0 = tt * 256 + xj * 32;
        const unsigned psrc = (unsigned)WS_P + (unsigned)(((tt * 4 + c4) * 8 + xj) * 8) * 9u * 1024u + l16;
        auto fin = [&](auto NDC) {
            constexpr int ND = NDC;
            u32x2 xv[ND][2];
            u32x4 vy[ND][8];
            f32x4 sacc[ND][2];
            auto sgroup = [&](auto G) {
                constexpr int g = G;
                bf16x8 B0[3], B1[3], A[3][ND];
                auto rdk = [&](auto KK) {
                    constexpr int ks = 6 * g + KK, sl = ks / 3;
                    constexpr int place = sl == 0 ? WQ1 : sl == 1 ? WQ2 : sl == 2 ? WQ0 : sl == 3 ? WQ4 : sl == 4 ? WQ1 : WQ2;
                    B0[KK % 3] = __builtin_bit_cast(bf16x8, lds_rd128(ldsbase + OTOP + ks * 1024 + l16));
                    B1[KK % 3] = __builtin_bit_cast(bf16x8, lds_rd128(ldsbase + WQ3 + ks * 1024 + l16));
#pragma unroll
                    for (int k = 0; k < ND; ++k) A[KK % 3][k] = tr_frag_o(arel + place + (ks % 3) * 32 * 288 + ((k == 0) ? wave : 8) * 32, 0, 8 * 288);
                };
                rdk(IC<0>{}); rdk(IC<1>{});
                sfor<6>([&](auto KK) {
                    constexpr int kk = KK, ks = 6 * g + kk;
                    if constexpr (kk + 2 < 6) rdk(IC<kk + 2>{});
                    SB();
#pragma unroll
                    for (int k = 0; k < ND; ++k) {
                        if constexpr (ks == 0) { sacc[k][0] = MFMA16(A[kk % 3][k], B0[kk % 3], ((f32x4){0.f, 0.f, 0.f, 0.f})); sacc[k][1] = MFMA16(A[kk % 3][k], B1[kk % 3], ((f32x4){0.f, 0.f, 0.f, 0.f})); }
                        else { sacc[k][0] = MFMA16(A[kk % 3][k], B0[kk % 3], sacc[k][0]); sacc[k][1] = MFMA16(A[kk % 3][k], B1[kk % 3], sacc[k][1]); }
                    }
                    SB();
                });
            };
            sgroup(IC<0>{});
            BAR();
            glds16x4(wob + (size_t)4 * 108 * 1152, vw, ldsbase + WQ1 + mb * 1024);
            wo_offsets(true);
            issue_wo(5, WQ2);
#pragma unroll
            for (int k = 0; k < ND; ++k) {
                const int dt = (k == 0) ? wave : 8;
#pragma unroll
                for (int a = 0; a < 2; ++a)
                    xv[k][a] = *(const u32x2*)((const char*)x + (size_t)(tok0 + a * 16 + t16) * 1152 + (c4 * 144 + dt * 16 + 4 * q4) * 2);
            }
            wait_flags(sy, SY_PF + (tt * 4 + c4) * 64 + (lane & 7), tag);
            STAMP(18);
#pragma unroll
            for (int k = 0; k < ND; ++k) {
                const int dt = (k == 0) ? wave : 8;
#pragma unroll
                for (int sj = 0; sj < 8; ++sj) vy[k][sj] = __builtin_amdgcn_raw_buffer_load_b128(rs, psrc + (sj * 9 + dt) * 1024, 0, 16);
            }
            BBAR();
            sgroup(IC<1>{});
            sgroup(IC<2>{});
#pragma unroll
            for (int k = 0; k < ND; ++k) {
                const int dt = (k == 0) ? wave : 8;
                float y[8];
#pragma unroll
                for (int e = 0; e < 8; ++e) y[e] = 0.f;
#pragma unroll
                for (int sj = 0; sj < 8; ++sj)
#pragma unroll
                    for (int e = 0; e < 4; ++e) { y[2 * e] += lo2f(vy[k][sj][e]); y[2 * e + 1] += hi2f(vy[k][sj][e]); }
#pragma unroll
                for (int a = 0; a < 2; ++a) {
                    float ov[4];
#pragma unroll
                    for (int r = 0; r < 4; ++r) {
                        const float yy = rbf(y[4 * a + r]);
                        const float g = rbf(yy * __builtin_amdgcn_rcpf(1.f + __builtin_amdgcn_exp2f(-yy * 1.4426950408889634f)));
                        const float pr = rbf(rbf(sacc[k][a][r]) * g);
                        const unsigned xw = xv[k][a][r >> 1];
                        ov[r] = ((r & 1) ? hi2f(xw) : lo2f(xw)) + pr;
                    }
                    u32x2 w = {pk2(ov[0], ov[1]), pk2(ov[2], ov[3])};
                    *(u32x2*)((char*)out + (size_t)(tok0 + a * 16 + t16) * 1152 + (c4 * 144 + dt * 16 + 4 * q4) * 2) = w;
                }
            }
        };
        if (wave == 0) fin(IC<2>{}); else fin(IC<1>{});
        if (bid == 0 && wave == 7) {
            for (unsigned spins = 0; spins < kSpinCap; ++spins) {
                bool ok = true;
#pragma unroll
                for (int k = 0; k < 4; ++k) ok &= flag_load(sy, SY_START + lane + 64 * k) == tag;
                if (__all(ok)) break;
                __builtin_amdgcn_s_sleep(1);
            }
            if (lane == 0) flag_store(sy, SY_GEN, tag);
        }
    }
    }
    STAMP(19);
}

extern "C" void kernel_launch(void* const* d_in, const int* in_sizes, int n_in, void* d_out, int out_size, void* d_ws, size_t ws_size,
                              hipStream_t stream) {
    const u16* x = (const u16*)d_in[0];
    const u16* wqkv = (const u16*)d_in[1];
    const u16* wo = (const u16*)d_in[2];
    const u16* wg = (const u16*)d_in[4];
    const u16* wu = (const u16*)d_in[5];
    const u16* wd = (const u16*)d_in[6];
    (void)hipFuncSetAttribute((const void*)mega_kernel, hipFuncAttributeMaxDynamicSharedMemorySize, LDS_BYTES);
    mega_kernel<<<256, 512, LDS_BYTES, stream>>>(x, wqkv, wo, wg, wu, wd, (char*)d_ws, (u16*)d_out);
}
```

```cpp
#include <hip/hip_runtime.h>
#include <hip/hip_bf16.h>
#include <stdint.h>
#include <utility>

typedef unsigned short u16;
typedef __bf16 bf16x8 __attribute__((ext_vector_type(8)));
typedef __bf16 bf16x2 __attribute__((ext_vector_type(2)));
typedef float f32x16 __attribute__((ext_vector_type(16)));
typedef float f32x4 __attribute__((ext_vector_type(4)));
typedef float f32x2 __attribute__((ext_vector_type(2)));
typedef short s16x4 __attribute__((ext_vector_type(4)));
typedef short s16x8 __attribute__((ext_vector_type(8)));
typedef unsigned u32x4 __attribute__((ext_vector_type(4)));
typedef unsigned u32x2 __attribute__((ext_vector_type(2)));
#define LAS __attribute__((address_space(3)))
#define GAS __attribute__((address_space(1)))
#define DI __device__ __forceinline__

constexpr int S = 2048, D = 576, F = 4608;
constexpr size_t WS_SYNC = 0;
constexpr int SY_GEN = 0;
constexpr int SY_START = 64;
constexpr int SY_XF = 512;
constexpr int SY_QF = SY_XF + 8 * 64;
constexpr int SY_HF = SY_QF + 8 * 64;
constexpr int SY_OF = SY_HF + 64 * 64;
constexpr int SY_PF = SY_OF + 8 * 64;
constexpr int SY_H2 = SY_PF + 32 * 64;
constexpr int SY_END = SY_H2 + 64 * 64;
static_assert(SY_END * 4 <= 131072, "sync area");
constexpr size_t WS_H = 131072;
constexpr size_t WS_H_BYTES = (size_t)64 * 288 * 1024;
constexpr size_t WS_QKV = WS_H + WS_H_BYTES;
constexpr size_t WS_QKV_BYTES = (size_t)3 * 32 * 2048 * 48;
constexpr size_t WS_O = WS_QKV + WS_QKV_BYTES + 4096;
constexpr size_t WS_O_BYTES = (size_t)128 * 18 * 1024;
constexpr size_t WS_P = WS_O + WS_O_BYTES;
constexpr size_t WS_P_BYTES = (size_t)8 * 4 * 8 * 8 * 9 * 1024;
constexpr size_t WS_X = WS_P + WS_P_BYTES;
constexpr size_t WS_X_BYTES = (size_t)64 * 36 * 1024;
constexpr size_t WS_USED = WS_X + WS_X_BYTES;

DI float lo2f(unsigned v) { return __uint_as_float(v << 16); }
DI float hi2f(unsigned v) { return __uint_as_float(v & 0xffff0000u); }
DI unsigned pk2(float a, float b) { f32x2 t = {a, b}; return __builtin_bit_cast(unsigned, __builtin_convertvector(t, bf16x2)); }
DI float rbf(float f) { return lo2f(pk2(f, 0.f)); }
DI float dot2(unsigned a, unsigned b, float c) { return __builtin_amdgcn_fdot2_f32_bf16(__builtin_bit_cast(bf16x2, a), __builtin_bit_cast(bf16x2, b), c, false); }
DI float xhalf_max(float v) { auto r = __builtin_amdgcn_permlane32_swap(__float_as_uint(v), __float_as_uint(v), false, false); return fmaxf(__uint_as_float(r[0]), __uint_as_float(r[1])); }
DI float lo_bcast(float v) { auto r = __builtin_amdgcn_permlane32_swap(__float_as_uint(v), __float_as_uint(v), false, false); return __uint_as_float(r[0]); }

DI void glds16(const void* sbase, unsigned voff, unsigned lds_dst) {
    asm volatile("s_mov_b32 m0, %2\n\ts_nop 0\n\tglobal_load_lds_dwordx4 %0, %1" ::"v"(voff), "s"(sbase), "s"(lds_dst) : "memory");
}
DI void glds16x3(const void* sbase, unsigned v0, unsigned v1, unsigned v2, unsigned lds_dst) {
    asm volatile("s_mov_b32 m0, %4\n\ts_nop 0\n\tglobal_load_lds_dwordx4 %0, %3\n\tglobal_load_lds_dwordx4 %1, %3 offset:1024\n\tglobal_load_lds_dwordx4 %2, %3 offset:2048"
                 ::"v"(v0), "v"(v1), "v"(v2), "s"(sbase), "s"(lds_dst) : "memory");
}
DI void glds16x4(const void* sbase, const unsigned (&v)[4], unsigned lds_dst) {
    asm volatile("s_mov_b32 m0, %5\n\ts_nop 0\n\tglobal_load_lds_dwordx4 %0, %4\n\tglobal_load_lds_dwordx4 %1, %4 offset:1024\n\tglobal_load_lds_dwordx4 %2, %4 offset:2048\n\tglobal_load_lds_dwordx4 %3, %4 offset:3072"
                 ::"v"(v[0]), "v"(v[1]), "v"(v[2]), "v"(v[3]), "s"(sbase), "s"(lds_dst) : "memory");
}
template <int OFF> DI void gload16_sc1(bf16x8& dst, unsigned voff, const char* sbase) {
    asm volatile("global_load_dwordx4 %0, %1, %2 offset:%3 sc1" : "=v"(dst) : "v"(voff), "s"(sbase), "i"(OFF) : "memory");
}
DI void gload4_sc1(unsigned& dst, unsigned voff, const void* sbase) {
    asm volatile("global_load_dword %0, %1, %2 sc1" : "=v"(dst) : "v"(voff), "s"(sbase) : "memory");
}
template <int N> DI void wait_vm_dep(bf16x8& v) { asm volatile("s_waitcnt vmcnt(%1)" : "+v"(v) : "i"(N)); }
template <int N> DI void wait_vm_dep2(bf16x8& v, bf16x8& w) { asm volatile("s_waitcnt vmcnt(%2)" : "+v"(v), "+v"(w) : "i"(N)); }
template <int N> DI void wait_vm_u(unsigned& v) { asm volatile("s_waitcnt vmcnt(%1)" : "+v"(v) : "i"(N)); }
template <int N> DI void wait_vm() { asm volatile("s_waitcnt vmcnt(%0)" ::"i"(N) : "memory"); }
#define WAIT_LGKM0() asm volatile("s_waitcnt lgkmcnt(0)" ::: "memory")
#define BAR() do { asm volatile("s_waitcnt lgkmcnt(0)" ::: "memory"); __builtin_amdgcn_s_barrier(); asm volatile("" ::: "memory"); } while (0)
#define BBAR() do { asm volatile("" ::: "memory"); __builtin_amdgcn_s_barrier(); asm volatile("" ::: "memory"); } while (0)
#define SB() __builtin_amdgcn_sched_barrier(0)
template <int... Is, class Fn> DI void sfor_impl(std::integer_sequence<int, Is...>, Fn&& f) { (f(std::integral_constant<int, Is>{}), ...); }
template <int N, class Fn> DI void sfor(Fn&& f) { sfor_impl(std::make_integer_sequence<int, N>{}, f); }
template <int V> using IC = std::integral_constant<int, V>;
DI void pin(bf16x8& v) { asm volatile("" : "+v"(v)); }
DI float glu_a(float g) { asm volatile("v_mul_f32 %0, 0xbfb8aa3b, %0\n\tv_exp_f32 %0, %0" : "+v"(g)); return g; }
DI float glu_b(float e, float u) { asm volatile("v_add_f32 %0, 1.0, %0\n\tv_rcp_f32 %0, %0\n\ts_nop 0\n\tv_mul_f32 %0, %0, %1" : "+v"(e) : "v"(u)); return e; }
template <int X, int ADD> DI unsigned xor_add(unsigned base) {
    unsigned r; asm volatile("v_xor_b32 %0, %2, %1\n\tv_add_u32 %0, %3, %0" : "=&v"(r) : "v"(base), "i"(X), "i"(ADD)); return r;
}
DI unsigned opaque(unsigned v) { asm volatile("" : "+v"(v)); return v; }
DI int fresh_lane() { int l; asm volatile("v_mbcnt_lo_u32_b32 %0, -1, 0\n\tv_mbcnt_hi_u32_b32 %0, -1, %0" : "=v"(l)); return l; }
DI bf16x8 tr_frag_o(unsigned base, int off0, int off1) {
    s16x4 lo = __builtin_amdgcn_ds_read_tr16_b64_v4i16((LAS s16x4*)((LAS char*)(size_t)base + off0));
    s16x4 hi = __builtin_amdgcn_ds_read_tr16_b64_v4i16((LAS s16x4*)((LAS char*)(size_t)base + off1));
    s16x8 f = __builtin_shufflevector(lo, hi, 0, 1, 2, 3, 4, 5, 6, 7);
    return __builtin_bit_cast(bf16x8, f);
}
DI u32x4 lds_rd128(unsigned a) { return *(LAS u32x4*)(size_t)a; }
DI unsigned lds_rd32(unsigned a) { return *(LAS unsigned*)(size_t)a; }
#define MFMA32(a, b, c) __builtin_amdgcn_mfma_f32_32x32x16_bf16((a), (b), (c), 0, 0, 0)
#define MFMA16(a, b, c) __builtin_amdgcn_mfma_f32_16x16x32_bf16((a), (b), (c), 0, 0, 0)

typedef __amdgpu_buffer_rsrc_t rsrc_t;
DI void st16_sc1(rsrc_t r, unsigned off, u32x4 v) { __builtin_amdgcn_raw_buffer_store_b128(v, r, off, 0, 16); }
DI void st16_sc1s(rsrc_t r, unsigned voff, unsigned soff, u32x4 v) {
    asm volatile("buffer_store_dwordx4 %0, %1, %2, %3 offen sc1\n\ts_nop 1" ::"v"(v), "v"(voff), "s"(r), "s"(soff) : "memory");
}
DI void st4_sc1(rsrc_t r, unsigned off, unsigned v) { __builtin_amdgcn_raw_buffer_store_b32(v, r, off, 0, 16); }
DI void flag_store(unsigned* sy, int idx, unsigned tag) { __hip_atomic_store((GAS unsigned*)sy + idx, tag, __ATOMIC_RELAXED, __HIP_MEMORY_SCOPE_AGENT); }
DI unsigned flag_load(const unsigned* sy, int idx) { return __hip_atomic_load((GAS unsigned*)sy + idx, __ATOMIC_RELAXED, __HIP_MEMORY_SCOPE_AGENT); }
constexpr unsigned kSpinCap = 1u << 20;
DI void wait_flags(const unsigned* sy, int idx, unsigned tag) {
    for (unsigned spins = 0; spins < kSpinCap; ++spins) {
        const unsigned v = flag_load(sy, idx);
        if (__all(v == tag)) break;
        __builtin_amdgcn_s_sleep(1);
    }
    asm volatile("" ::: "memory");
}

#define STAMP(i) do {} while (0)

template <int CTRL, int RMASK> DI float dpp_add(float v) {
    return v + __int_as_float(__builtin_amdgcn_update_dpp(0, __float_as_int(v), CTRL, RMASK, 0xf, true));
}
DI float wave_sum(float v) {
    v = dpp_add<0x111, 0xf>(v); v = dpp_add<0x112, 0xf>(v); v = dpp_add<0x114, 0xf>(v); v = dpp_add<0x118, 0xf>(v);
    v = dpp_add<0x142, 0xa>(v); v = dpp_add<0x143, 0xc>(v);
    return __int_as_float(__builtin_amdgcn_readlane(__float_as_int(v), 63));
}

constexpr int SLOT = 24576;
constexpr int SPARE = SLOT * 6;
constexpr int LDS_BYTES = 163840;
constexpr int KPOS = 4 * SLOT, VPOS = 5 * SLOT;
constexpr int WSL = 27648;
constexpr int WQ0 = 0, WQ1 = WSL, WQ2 = 2 * WSL, WQ3 = 3 * WSL, WQ4 = 4 * WSL, OTOP = 5 * WSL;
static_assert(WQ3 <= KPOS && OTOP + 18 * 1024 <= LDS_BYTES, "lds");

struct Ctx {
    const char* wq; const char* wgu;
    unsigned vq0, vq1, vq2, vg0, vg1, vg2;
    unsigned ldsbase;
    int wave; int P0;
};
DI void issue_q(const Ctx& c, int chunk, int pos) { glds16x3(c.wq + (size_t)(chunk * 192) * 3456, c.vq0, c.vq1, c.vq2, c.ldsbase + pos * SLOT + c.wave * 3072); }
DI void issue_gu(const Ctx& c, int panel, int chunk, int pos) {
    glds16x3(c.wgu + (size_t)(chunk * 96) * 9216 + (size_t)(c.P0 + panel) * 128, c.vg0, c.vg1, c.vg2, c.ldsbase + pos * SLOT + c.wave * 3072);
}

constexpr int PF1 = 3;
DI void issue_slot(const Ctx& c, int m) {
    if (m < 6) issue_gu(c, 1, m, m % 6);
    else if (m < 9) issue_q(c, m - 6, m % 6);
    else if (m < 15) issue_gu(c, 0, m - 9, m % 6);
    else issue_gu(c, 2, m - 15, m % 6);
}

struct Hand {
    unsigned* sy; unsigned tag; rsrc_t rs; int tt, j, c4, cgrp;
    const char* kb; const char* vb; const char* qb;
};

constexpr int NSTEPS = 396;
struct Step { short slot, ks, t, set, tau, first, fmt; };
struct Sched {
    Step st[NSTEPS];
    short fill[NSTEPS + 100][3];
    short nrows;
    short qn;
    short qent[24][5];
    short qlast;
};
template <int C4> constexpr Sched make_sched() {
    Sched S{};
    const int NTa = 4 - C4, NTc = 1 + C4;
    int setsA[4] = {2, 3, 0, 1};
    int setsC[4] = {0, 0, 0, 0};
    {
        int k = 0;
        for (int s = 0; s < 4; ++s) { int order[4] = {3, 0, 1, 2}; int cand = order[s]; bool used = false; for (int t = 0; t < NTa; ++t) used |= (setsA[t] == cand); if (!used && k < NTc - 1) setsC[k++] = cand; }
        setsC[NTc - 1] = setsA[0];
    }
    int n = 0, lastStep[11] = {};
    for (int slot = 0; slot < 21; ++slot) {
        int pass = slot < 6 ? 0 : slot < 9 ? 1 : slot < 15 ? 2 : 3;
        int i = pass == 0 ? slot : pass == 1 ? slot - 6 : pass == 2 ? slot - 9 : slot - 15;
        int nt = pass == 0 ? 4 : pass == 1 ? 2 : pass == 2 ? NTa : NTc;
        int kps = pass == 1 ? 12 : 6;
        int nsl = pass == 1 ? 3 : 6;
        int tau0 = pass == 0 ? 0 : pass == 1 ? 4 : pass == 2 ? 6 : 6 + NTa;
        bool tmajor = (i == nsl - 1) || (i == 0 && pass > 0);
        for (int k = 0; k < nt * kps; ++k) {
            int t = tmajor ? k / kps : k % nt, s = tmajor ? k % kps : k / nt;
            Step e{};
            e.slot = slot; e.ks = i * kps + s; e.t = t; e.tau = tau0 + t; e.fmt = pass == 1; e.first = (e.ks == 0);
            e.set = pass == 0 ? t : pass == 1 ? t : pass == 2 ? setsA[t] : setsC[t];
            S.st[n] = e; lastStep[e.tau] = n; ++n;
        }
    }
    const int delta = C4 == 0 ? 0 : C4 == 1 ? 6 : C4 == 2 ? 4 : 2;
    int qn = 0;
    for (int sl = 0; sl < 3; ++sl)
        for (int t = 0; t < 2; ++t)
            for (int r = 0; r < 16; r += 2) {
                int m = 0, d0 = 0;
                for (int hh = 1; hh >= 0; --hh) { int nn = 32 * t + (r & 3) + 8 * (r >> 2) + 4 * hh - delta; if (nn >= 0 && nn < 54 && nn / 18 == sl) { m |= 1 << hh; d0 = nn % 18 - 4 * hh; } }
                if (m) { S.qent[qn][0] = t; S.qent[qn][1] = r; S.qent[qn][2] = sl; S.qent[qn][3] = m; S.qent[qn][4] = d0; ++qn; }
            }
    S.qn = qn;
    int freeg = 0;
    auto put = [&](int g, int code) { for (int k = 0; k < 3; ++k) if (S.fill[g][k] == 0) { S.fill[g][k] = code; return; } S.fill[g][0] = -1; };
    for (int tau = 0; tau < 11; ++tau) {
        if (tau == 4) continue;
        int ready = lastStep[tau] + 3;
        int g = ready > freeg ? ready : freeg;
        int dl = 100000;
        for (int k = lastStep[tau] + 1; k < NSTEPS; ++k) if (S.st[k].first && (S.st[k].set == S.st[lastStep[tau]].set || (tau == 5 && S.st[k].set == S.st[lastStep[4]].set))) { dl = k; break; }
        int ops[40] = {}, cnt = 0;
        if (tau == 5) {
            for (int sl = 0; sl < 3; ++sl) {
                for (int e = 0; e < qn; ++e) if (S.qent[e][2] == sl) ops[cnt++] = 2000 + e;
                ops[cnt++] = 2100 + sl; ops[cnt++] = 2110 + sl;
            }
        } else {
            const int b = 1000 + tau * 32;
            ops[cnt++] = b + 0;
            for (int j = 0; j < 7; ++j) { ops[cnt++] = b + j + 1; ops[cnt++] = b + 8 + j; }
            ops[cnt++] = b + 15; ops[cnt++] = b + 16; ops[cnt++] = b + 17;
        }
        int avail = dl - g, dbl = cnt > avail ? cnt - avail : 0;
        if (tau == 5 && dbl < cnt / 2) dbl = cnt / 2;
        int k = 0;
        while (k < cnt) { put(g, ops[k++]); if (dbl > 0 && k < cnt) { put(g, ops[k++]); --dbl; } ++g; }
        if (tau == 5) { for (int q = 0; q < NSTEPS + 100; ++q) for (int z = 0; z < 3; ++z) if (S.fill[q][z] == 2112) S.qlast = q; }
        freeg = g;
    }
    S.nrows = freeg;
    return S;
}
template <int C4> struct SchedOf { static constexpr Sched SC = make_sched<C4>(); };
template <int C4> constexpr int set_of(int tau) { for (int g = 0; g < NSTEPS; ++g) if (SchedOf<C4>::SC.st[g].tau == tau) return SchedOf<C4>::SC.st[g].set; return 0; }
constexpr int xpos(int s) { return s < 6 ? -1 : 4 * ((s - 6) / 3) + 2; }
constexpr int dpos(int d) {
    return d == 2 ? -1 : d == 3 ? 4 * 3 + 1 : d == 4 ? 4 * 9 + 1 : d == 5 ? 4 * 15 + 1 : d == 6 ? 4 * 21 + 1 : d == 7 ? 4 * 27 + 1 : d == 8 ? 24 * 5 + 1
         : d == 9 || d == 10 ? 24 * 6 + 1 : d == 11 || d == 12 ? 24 * 7 + 1 : 24 * (d - 5) + 1;
}
constexpr bool d_after(int e, int d) { return dpos(e) > dpos(d) || (dpos(e) == dpos(d) && e > d); }
constexpr int younger_than_x(int s, int now) {
    int n = 0;
    for (int t = s + 1; t < 36; ++t) if (xpos(t) < now) ++n;
    for (int d = 2; d <= 20; ++d) if (dpos(d) < now && (dpos(d) > xpos(s) || (dpos(d) == xpos(s) && d == 2))) n += 3;
    return n;
}
constexpr int younger_than_d(int d, int now) {
    int n = 0;
    for (int t = 0; t < 36; ++t) if (xpos(t) < now && xpos(t) > dpos(d)) ++n;
    for (int e = 2; e <= 20; ++e) if (dpos(e) < now && d_after(e, d)) n += 3;
    return n;
}

template <int C4>
DI void gemm1(const Ctx& c, const Hand& hd, char* ws, int tile32) {
#define S (SchedOf<C4>::SC)
    constexpr int NTa = 4 - C4;
    constexpr int NK = (C4 <= 1) ? 17 : (C4 == 2 ? 18 : 19), NV = (C4 <= 2) ? 18 : 19;
    constexpr int NCHK = C4 == 0 ? 14 : C4 == 1 ? 15 : C4 == 2 ? 17 : 18;
    constexpr int NQF = S.st[S.qlast < NSTEPS ? S.qlast : NSTEPS - 1].slot + 2;
    static_assert(NQF <= 13 && NQF >= 9, "qkv publish");
    unsigned Lgu, Lq = 0, l16;
    {
        const int lane = fresh_lane();
        const int hh = lane >> 5, blk = (lane >> 4) & 1, q = (lane & 15) >> 2, pp = lane & 3;
        Lgu = c.ldsbase + (unsigned)(blk * 12288 + (8 * hh + q) * 128 + 8 * pp + 32 * (((q >> 1) & 1) | (blk << 1)));
        l16 = lane * 16;
    }
    const unsigned hbs = (unsigned)WS_H + ((unsigned)tile32 * 288u + (unsigned)c.P0 * 4u) * 1024u;
    const unsigned stg = c.ldsbase + SPARE + c.wave * 2048;
    const unsigned qds = (unsigned)WS_QKV + (unsigned)((hd.cgrp * 3) * 2048 + tile32 * 32) * 48u;
    unsigned stw = 0;
    const char* xs = ws + WS_X + (size_t)tile32 * 36 * 1024;
    bf16x8 xf[36];
    f32x16 acc[4];
    unsigned cur[4];
    bf16x8 A[PF1 + 1];
    unsigned qflag = 0, pl0 = 0, pl1 = 0;
    sfor<6>([&](auto SI) { constexpr int s = SI; gload16_sc1<(s & 3) * 1024>(xf[s], l16, xs + (s >> 2) * 4096); });
    issue_slot(c, 2);
    auto rd = [&](auto IDX) {
        constexpr Step e = S.st[IDX];
        constexpr int kl = e.fmt ? e.ks % 12 : e.ks % 6, pos = e.slot % 6;
        constexpr int col = e.fmt ? e.t : (e.slot >= 9 && e.slot < 15 ? C4 + e.t : e.t);
        if constexpr (e.fmt && e.ks == 0 && e.t == 0) {
            const int lane = fresh_lane();
            const int hh = lane >> 5, blk = (lane >> 4) & 1, q = (lane & 15) >> 2, pp = lane & 3;
            Lq = c.ldsbase + (unsigned)((8 * hh + q) * 128 + 8 * pp + 32 * (blk ^ (((q >> 1) & 1) << 1)));
        }
        if constexpr (kl == 0) cur[e.t] = e.fmt ? xor_add<(col << 6), pos * SLOT>(Lq) : xor_add<(col << 5), pos * SLOT>(Lgu);
        return tr_frag_o(cur[e.t], kl * 2048, kl * 2048 + 512);
    };
    auto filler = [&](auto CODE) {
        constexpr int code = CODE;
        if constexpr (code >= 1000 && code < 2000) {
            constexpr int tau = (code - 1000) / 32, j = (code - 1000) % 32;
            constexpr int realset = set_of<C4>(tau);
            constexpr int fgo = tau < 4 ? 4 + tau : (tau < 6 + NTa ? C4 + tau - 6 : 8 + tau - 6 - NTa);
            if constexpr (j < 8) acc[realset][j] = glu_a(acc[realset][j]);
            else if constexpr (j < 16) acc[realset][j - 8] = glu_b(acc[realset][j - 8], acc[realset][j]);
            else if constexpr (j == 16) {
                pl0 = pk2(acc[realset][0], acc[realset][1]);
                pl1 = pk2(acc[realset][2], acc[realset][3]);
            } else {
                u32x4 w = {pl0, pl1, pk2(acc[realset][4], acc[realset][5]), pk2(acc[realset][6], acc[realset][7])};
                st16_sc1s(hd.rs, l16, hbs + fgo * 1024, w);
            }
        } else if constexpr (code >= 2000 && code < 2100) {
            constexpr int e = code - 2000, t = S.qent[e][0], r = S.qent[e][1], m = S.qent[e][3], d0 = S.qent[e][4];
            if constexpr (e == 0) { const int ln = fresh_lane(); stw = opaque(stg + (ln & 31) * 48 + ((ln >> 5) << 3)); }
            const float qs = (hd.cgrp * 3 + S.qent[e][2] < 32) ? 0.23570226039551584f * 1.4426950408889634f : 1.f;
            const unsigned v = pk2(acc[t][r] * qs, acc[t][r + 1] * qs);
            if constexpr (m == 3) *(LAS unsigned*)(size_t)(stw + 2 * d0) = v;
            else if ((int)((stw >> 3) & 1) == (m >> 1)) *(LAS unsigned*)(size_t)(stw + 2 * d0) = v;
        } else if constexpr (code >= 2100 && code < 2110) {
            constexpr int sl = code - 2100;
            if (((stw >> 3) & 1) == 0) *(LAS unsigned*)(size_t)(stw + 36) = (hd.cgrp * 3 + sl >= 64) ? 0x00003F80u : 0u;
            else *(LAS u32x2*)(size_t)(stw + 32) = (u32x2){0u, 0u};
        } else if constexpr (code >= 2110) {
            constexpr int sl = code - 2110;
            const u32x4 w0 = lds_rd128(stg + l16);
            st16_sc1s(hd.rs, l16, qds + (unsigned)sl * 2048u * 48u, w0);
            if (fresh_lane() < 32) { const u32x4 w1 = lds_rd128(stg + 1024 + l16); st16_sc1s(hd.rs, l16, qds + (unsigned)sl * 2048u * 48u + 1024, w1); }
        }
    };
    sfor<NSTEPS>([&](auto IDX) {
        constexpr int idx = IDX;
        constexpr Step e = S.st[idx];
        constexpr bool top = (idx == 0) || (S.st[idx > 0 ? idx - 1 : 0].slot != e.slot);
        if constexpr (top) {
            constexpr int n = e.slot;
            if constexpr (n == 5) {
                wait_vm<(younger_than_x(35, 120) < younger_than_d(6, 120) ? younger_than_x(35, 120) : younger_than_d(6, 120))>();
                pin(xf[30]); pin(xf[31]); pin(xf[32]); pin(xf[33]); pin(xf[34]); pin(xf[35]);
            } else if constexpr (n == NQF) wait_vm<3>();
            else if constexpr (n >= 1 && n <= 16) wait_vm<younger_than_d(n + 1, 24 * n)>();
            else if constexpr (n == 17) wait_vm<6>();
            else if constexpr (n == 18) wait_vm<3 + (NK <= 17 ? 3 : 0)>();
            else if constexpr (n == 19) wait_vm<(NK <= 18 ? 3 : 0) + (NV <= 18 ? 5 : 0)>();
            if constexpr (n == NCHK) {
                if (c.wave == 0) {
                    wait_vm_u<(n + 4 <= 20 ? 3 : 0)>(qflag);
                    if (!__all(qflag == hd.tag)) { const int ln = fresh_lane(); wait_flags(hd.sy, SY_QF + ((ln >= 32 && hd.tt > 0) ? hd.tt - 1 : hd.tt) * 64 + (ln & 31), hd.tag); }
                    asm volatile("buffer_inv sc1" ::: "memory");
                }
            }
            if constexpr (n == NK) { if (c.wave == 0) wait_vm<(NCHK + 5 <= 20 ? 3 * (20 - (NCHK + 5) + 1) : 0)>(); }
            if constexpr (n > 0) BBAR();
            if constexpr (n == NQF) { if (c.wave == 0 && fresh_lane() == 0) flag_store(hd.sy, SY_QF + hd.tt * 64 + hd.cgrp, hd.tag); }
            if constexpr (n == NCHK - 1) { if (c.wave == 0) { const int lane = fresh_lane(); gload4_sc1(qflag, (unsigned)(SY_QF + ((lane >= 32 && hd.tt > 0) ? hd.tt - 1 : hd.tt) * 64 + (lane & 31)) * 4u, hd.sy); } }
            sfor<19>([&](auto DD) { constexpr int d = DD + 2; if constexpr (dpos(d) == 24 * n + 1 && n >= 5) issue_slot(c, d); });
            if constexpr (n == NK || n == NV) {
#pragma unroll
                for (int kv = 0; kv < 2; ++kv) {
                    if ((kv == 0 && n == NK) || (kv == 1 && n == NV)) {
#pragma unroll
                        for (int k = 0; k < 3; ++k) {
                            const int m = c.wave * 3 + k;
                            int off = (hd.tt * 256 - 256) * 48 + m * 1024;
                            if (off < 0) off = m * 1024;
                            glds16((kv == 0 ? hd.kb : hd.vb) + off, l16, c.ldsbase + (kv == 0 ? KPOS : VPOS) + m * 1024);
                        }
                    }
                }
            }
            if constexpr (n == NV) {
#pragma unroll
                for (int k = 0; k < 2; ++k) {
                    int m = c.wave * 2 + k; if (m > 11) m = 11;
                    glds16(hd.qb + m * 1024, l16, c.ldsbase + SPARE + m * 1024);
                }
            }
            if constexpr (idx == 0) sfor<PF1>([&](auto J) { A[J] = rd(J); });
        }
        if constexpr (idx + PF1 < NSTEPS) A[(idx + PF1) % (PF1 + 1)] = rd(IC<idx + PF1>{});
        if constexpr (e.slot < 5 && e.t == 0) {
            sfor<6>([&](auto DD) { constexpr int d = DD + 3; if constexpr (d <= 7 && dpos(d) == 4 * e.ks + 1) issue_slot(c, d); });
            sfor<3>([&](auto JJ) { constexpr int sx = 6 + 3 * e.ks + JJ; if constexpr (sx < 36) gload16_sc1<(sx & 3) * 1024>(xf[sx], l16, xs + (sx >> 2) * 4096); });
            wait_vm_dep<younger_than_x(e.ks, 4 * e.ks + 3)>(xf[e.ks]);
        }
        SB();
        if constexpr (e.first) {
            f32x16 z;
#pragma unroll
            for (int r = 0; r < 16; ++r) z[r] = 0.f;
            acc[e.set] = MFMA32(A[idx % (PF1 + 1)], xf[e.ks], z);
        } else acc[e.set] = MFMA32(A[idx % (PF1 + 1)], xf[e.ks], acc[e.set]);
        SB();
        if constexpr (S.fill[idx][0] != 0) {
            filler(IC<S.fill[idx][0]>{});
            if constexpr (S.fill[idx][1] != 0) filler(IC<S.fill[idx][1]>{});
            if constexpr (S.fill[idx][2] != 0) filler(IC<S.fill[idx][2]>{});
            SB();
        }
    });
    sfor<S.nrows - NSTEPS>([&](auto R) {
        constexpr int g = NSTEPS + R;
        if constexpr (S.fill[g][0] != 0) filler(IC<S.fill[g][0]>{});
        if constexpr (S.fill[g][1] != 0) filler(IC<S.fill[g][1]>{});
        if constexpr (S.fill[g][2] != 0) filler(IC<S.fill[g][2]>{});
    });
}
#undef S

constexpr int g2_ks(int u) { constexpr int o[18] = {0, 1, 2, 3, 5, 6, 7, 9, 10, 11, 14, 15, 4, 8, 12, 13, 16, 17}; return o[u]; }
constexpr int g2_place(int sl) { return sl == 0 ? WQ0 : sl == 1 ? WQ1 : sl == 2 ? WQ2 : sl == 3 ? WQ3 : sl == 4 ? WQ4 : WQ0; }
constexpr int ewait(int u) { constexpr int c[8] = {0, 4, 8, 16, 16, 20, 24, 24}; return 2 * (11 - u) + 2 + c[u]; }

__global__ __launch_bounds__(512, 2) void mega_kernel(const u16* __restrict__ x, const u16* __restrict__ wqkv, const u16* __restrict__ wo,
                                                      const u16* __restrict__ wg, const u16* __restrict__ wu, const u16* __restrict__ wd,
                                                      char* __restrict__ ws, u16* __restrict__ out) {
    extern __shared__ __attribute__((aligned(16))) char smem[];
    const int tid = threadIdx.x;
    const int wave = __builtin_amdgcn_readfirstlane(tid >> 6);
    const int bid = blockIdx.x, xj = bid & 7, loc = bid >> 3;
    const int c4 = loc & 3, tt = loc >> 2, cgrp = xj * 4 + c4;
    const unsigned ldsbase = (unsigned)(size_t)(LAS char*)smem;
    unsigned* sy = (unsigned*)(ws + WS_SYNC);
    const rsrc_t rs = __builtin_amdgcn_make_buffer_rsrc(ws, 0, (unsigned)WS_USED, 0x00020000);
    unsigned tag, warm0, warm1;
    Ctx c;

    STAMP(0);
    {
        const int lane = tid & 63;
        const int xrow = bid * 8 + wave;
        const char* xr = (const char*)x + (size_t)xrow * 1152;
        u32x4 xv0 = *(const u32x4*)(xr + lane * 16);
        u32x4 xv1 = *(const u32x4*)(xr + 1024 + (lane & 7) * 16);
        const unsigned gen = flag_load(sy, SY_GEN);
        const int qa0 = (108 * cgrp) & ~15;
        c.wq = (const char*)wqkv + qa0; c.wgu = (const char*)((wave >> 2) ? wu : wg);
        c.ldsbase = ldsbase; c.wave = wave; c.P0 = (9 * cgrp) >> 2;
        {
            const int lrow = lane >> 3, c16 = lane & 7, bsel = wave >> 2;
            const int swgu = ((lrow >> 1) & 1) | (bsel << 1);
            const int chgu = (((c16 >> 1) ^ swgu) << 1) | (c16 & 1);
            c.vg0 = (unsigned)((24 * (wave & 3) + lrow) * 9216 + chgu * 16); c.vg1 = c.vg0 + 8 * 9216 - 1024; c.vg2 = c.vg0 + 16 * 9216 - 2048;
            const int swq = ((lrow >> 1) & 1) << 1;
            int chq = (((c16 >> 1) ^ swq) << 1) | (c16 & 1);
            if (cgrp == 31 && chq == 7) chq = 6;
            c.vq0 = (unsigned)((24 * wave + lrow) * 3456 + chq * 16); c.vq1 = c.vq0 + 8 * 3456 - 1024; c.vq2 = c.vq0 + 16 * 3456 - 2048;
        }
        tag = (unsigned)__builtin_amdgcn_readfirstlane((int)(gen * 0x9E3779B1u + 0x7F4A7C15u));
        float s1 = 0.f;
        if (lane >= 8) xv1 = (u32x4){0u, 0u, 0u, 0u};
#pragma unroll
        for (int k = 0; k < 4; ++k) { s1 = dot2(xv0[k], 0x3f803f80u, s1); s1 = dot2(xv1[k], 0x3f803f80u, s1); }
        const float meanf = wave_sum(s1) * (1.f / 576.f);
        float s2 = 0.f;
#pragma unroll
        for (int k = 0; k < 4; ++k) {
            float a = lo2f(xv0[k]) - meanf, b = hi2f(xv0[k]) - meanf; s2 += a * a + b * b;
            if (lane < 8) { a = lo2f(xv1[k]) - meanf; b = hi2f(xv1[k]) - meanf; s2 += a * a + b * b; }
        }
        const float var = rbf(wave_sum(s2) * (1.f / 576.f));
        const float mean = rbf(meanf);
        const float rden = 1.f / rbf(sqrtf(rbf(var + 1e-5f)));
        u32x4 o0, o1;
#pragma unroll
        for (int k = 0; k < 4; ++k) {
            o0[k] = pk2(rbf(lo2f(xv0[k]) - mean) * rden, rbf(hi2f(xv0[k]) - mean) * rden);
            o1[k] = pk2(rbf(lo2f(xv1[k]) - mean) * rden, rbf(hi2f(xv1[k]) - mean) * rden);
        }
        *(LAS u32x4*)(size_t)(ldsbase + SPARE + (lane * 8 + wave) * 16) = o0;
        if (lane < 8) *(LAS u32x4*)(size_t)(ldsbase + SPARE + ((64 + lane) * 8 + wave) * 16) = o1;
        BAR();
        const unsigned xd = (unsigned)WS_X + (unsigned)(bid >> 2) * 36u * 1024u;
        const int t0 = (bid & 3) * 8;
        {
            const int i = tid, cc = i >> 3, tk = i & 7;
            st16_sc1(rs, xd + ((cc >> 1) * 64 + (cc & 1) * 32 + t0 + tk) * 16, lds_rd128(ldsbase + SPARE + i * 16));
        }
        {
            const int i = 512 + lane, cc = i >> 3, tk = i & 7;
            st16_sc1(rs, wave == 0 ? xd + ((cc >> 1) * 64 + (cc & 1) * 32 + t0 + tk) * 16 : 0xFFFFFF00u, lds_rd128(ldsbase + SPARE + i * 16));
        }
        issue_slot(c, 0); issue_slot(c, 1);
        {
            const unsigned g = (unsigned)(bid * 512 + tid);
            const char* p0 = g < 41472u ? (const char*)wg + (size_t)g * 128 : (g < 82944u ? (const char*)wu + (size_t)(g - 41472u) * 128 : (const char*)wqkv + (size_t)((g - 82944u) % 15552u) * 128);
            const char* p1 = g < 41472u ? (const char*)wd + (size_t)g * 128 : (const char*)wo + (size_t)((g - 41472u) % 5184u) * 128;
            asm volatile("global_load_dword %0, %1, off" : "=v"(warm0) : "v"(p0) : "memory");
            asm volatile("global_load_dword %0, %1, off" : "=v"(warm1) : "v"(p1) : "memory");
        }
        wait_vm<8>();
        BBAR();
        if (tid == 0) { flag_store(sy, SY_XF + (bid >> 5) * 64 + (bid & 31), tag); flag_store(sy, SY_START + bid, tag); }
    }
    STAMP(1);
    Hand hd;
    hd.sy = sy; hd.tag = tag; hd.rs = rs; hd.tt = tt; hd.j = xj; hd.c4 = c4; hd.cgrp = cgrp;
    hd.kb = ws + WS_QKV + ((size_t)(32 + cgrp) * 2048) * 48; hd.vb = ws + WS_QKV + ((size_t)(64 + cgrp) * 2048) * 48;
    hd.qb = ws + WS_QKV + ((size_t)cgrp * 2048 + tt * 256) * 48;

    if (wave == 0) wait_flags(sy, SY_XF + tt * 64 + (fresh_lane() & 31), tag);
    wait_vm<0>();
    asm volatile("" ::"v"(warm0), "v"(warm1));
    BBAR();
    STAMP(2);
    const int tile32 = tt * 8 + wave;
    if (c4 == 0) gemm1<0>(c, hd, ws, tile32);
    else if (c4 == 1) gemm1<1>(c, hd, ws, tile32);
    else if (c4 == 2) gemm1<2>(c, hd, ws, tile32);
    else gemm1<3>(c, hd, ws, tile32);
    STAMP(7);

    {
    const int lane = fresh_lane();
    const int hh = lane >> 5, blk = (lane >> 4) & 1, q = (lane & 15) >> 2, pp = lane & 3;
    const unsigned l16 = lane * 16;
    const bool tid0 = (wave == 0) && (lane == 0);
    const char* wdb = (const char*)wd + (size_t)(xj * 576) * 1152 + c4 * 288;
    const int mb = wave < 6 ? 4 * wave : 23;
    unsigned vo[4];
#pragma unroll
    for (int i = 0; i < 4; ++i) {
        const int n = (mb + i) * 64 + lane, row = n / 18, cc = n - row * 18;
        vo[i] = (unsigned)(row * 1152 + cc * 16 - 1024 * i);
    }
    auto issue_wd = [&](int slot, int dst) { glds16x4(wdb + (size_t)slot * 96 * 1152, vo, ldsbase + dst + mb * 1024); };
    const int q4 = lane >> 4, t16 = lane & 15, qq = (lane & 15) >> 2;
    const char* hsrc = ws + WS_H + ((size_t)tile32 * 288 + xj * 36) * 1024;
    const unsigned hvo = (unsigned)(((q4 >> 1) * 64 + (q4 & 1) * 32 + t16) * 16);
    bf16x8 hf[18][2];
    auto hload = [&](auto UI) { constexpr int u = UI, s = g2_ks(u);
        gload16_sc1<(s & 1) * 2048>(hf[u][0], hvo, hsrc + (s >> 1) * 4096);
        gload16_sc1<(s & 1) * 2048 + 256>(hf[u][1], hvo, hsrc + (s >> 1) * 4096); };
    unsigned hflag = 0;
    const int hfi = (xj * 8 + tt) * 64;
    {
        if (c4 == 0) wait_vm<1>(); else if (c4 == 1) wait_vm<2>(); else if (c4 == 2) wait_vm<3>(); else wait_vm<4>();
        BBAR();
        if (tid0) flag_store(sy, SY_HF + hfi + c4, tag);
        issue_wd(0, WQ0); issue_wd(1, WQ1); issue_wd(2, WQ2);
        STAMP(8);
        const int t = lane & 31;
        const int q0 = tt * 256 + wave * 32;
        bf16x8 Qf0, Qf1;
        {
            const unsigned qa = ldsbase + SPARE + (wave * 32 + t) * 48;
            Qf0 = __builtin_bit_cast(bf16x8, lds_rd128(qa + 16 * hh));
            const unsigned qv1 = lds_rd32(qa + 32);
            u32x4 q1 = {hh == 0 ? qv1 : 0u, 0u, 0u, 0u};
            Qf1 = __builtin_bit_cast(bf16x8, q1);
        }
        const unsigned kaddr = ldsbase + KPOS + (wave * 32 + t) * 48;
        const unsigned vaddr = ldsbase + VPOS + (wave * 32 + 4 * hh + q) * 48 + 32 * blk + 8 * pp;
        const float NEG = -__builtin_inff();
        f32x16 o;
        auto attend = [&](auto FULLC) {
            constexpr bool FULL = FULLC;
            const int ktmin = FULL ? 0 : 8 - wave;
            f32x16 sc[9];
#pragma unroll
            for (int kt = 0; kt < 9; ++kt) {
                if (FULL || kt >= ktmin) {
                    u32x4 k0 = lds_rd128(kaddr + kt * 1536 + 16 * hh);
                    u32x4 k1 = {lds_rd32(kaddr + kt * 1536 + 32), 0u, 0u, 0u};
                    f32x16 z;
#pragma unroll
                    for (int r = 0; r < 16; ++r) z[r] = 0.f;
                    z = MFMA32(__builtin_bit_cast(bf16x8, k0), Qf0, z);
                    sc[kt] = MFMA32(__builtin_bit_cast(bf16x8, k1), Qf1, z);
                } else {
#pragma unroll
                    for (int r = 0; r < 16; ++r) sc[kt][r] = NEG;
                }
            }
#pragma unroll
            for (int r = 0; r < 16; ++r) {
                const int kk = (r & 3) + 8 * (r >> 2) + 4 * hh;
                if (!(kk > t)) sc[0][r] = NEG;
                if (!(kk <= t)) sc[8][r] = NEG;
            }
            float m = NEG;
#pragma unroll
            for (int kt = 0; kt < 9; ++kt)
#pragma unroll
                for (int r = 0; r < 16; ++r) m = fmaxf(m, sc[kt][r]);
            m = xhalf_max(m);
            if (wave == 0) gload4_sc1(hflag, (unsigned)(SY_HF + hfi + (lane & 3)) * 4u, sy);
#pragma unroll
            for (int r = 0; r < 16; ++r) o[r] = 0.f;
            sfor<9>([&](auto KT) {
                constexpr int kt = KT;
                if constexpr (kt == 2) {
                    wait_vm<12>();
                    if (wave == 0) {
                        wait_vm_u<0>(hflag);
                        if (!__all(hflag == tag)) wait_flags(sy, SY_HF + hfi + (lane & 3), tag);
                    }
                    BBAR();
                    if (tid0) flag_store(sy, SY_H2 + hfi + c4, tag);
                }
                if constexpr (kt == 6) { if (wave == 0) gload4_sc1(hflag, (unsigned)(SY_H2 + hfi + (lane & 3)) * 4u, sy); }
                if (FULL || kt >= ktmin) {
                    unsigned pk[8];
#pragma unroll
                    for (int r = 0; r < 16; r += 2)
                        pk[r >> 1] = pk2(__builtin_amdgcn_exp2f(sc[kt][r] - m), __builtin_amdgcn_exp2f(sc[kt][r + 1] - m));
#pragma unroll
                    for (int s = 0; s < 2; ++s) {
                        u32x4 pb = {pk[4 * s], pk[4 * s + 1], pk[4 * s + 2], pk[4 * s + 3]};
                        bf16x8 A = tr_frag_o(vaddr, (kt * 32 + 16 * s) * 48, (kt * 32 + 16 * s + 8) * 48);
                        o = MFMA32(A, __builtin_bit_cast(bf16x8, pb), o);
                    }
                }
                if constexpr (kt >= 3) { SB(); hload(IC<2 * (kt - 3)>{}); hload(IC<2 * (kt - 3) + 1>{}); SB(); }
            });
        };
        if (tt > 0) attend(std::true_type{}); else attend(std::false_type{});
        const float inv = 1.f / lo_bcast(o[10]);
        const int tile16 = (q0 >> 4) + ((lane >> 4) & 1);
        const unsigned ob = (unsigned)WS_O + (unsigned)(tile16 * 18) * 1024u + t16 * 16;
        u32x4 w = {pk2(o[0] * inv, o[1] * inv), pk2(o[2] * inv, o[3] * inv), pk2(o[4] * inv, o[5] * inv), pk2(o[6] * inv, o[7] * inv)};
        st16_sc1(rs, ob + (cgrp >> 1) * 1024 + (2 * (cgrp & 1) + hh) * 256, w);
        st4_sc1(rs, hh == 0 ? ob + (16 + (cgrp >> 4)) * 1024 + ((cgrp >> 2) & 3) * 256 + (cgrp & 3) * 4 : 0xFFFFFFF0u, pk2(o[8] * inv, o[9] * inv));
        if (wave == 0) {
            wait_vm_u<14>(hflag);
            if (!__all(hflag == tag)) wait_flags(sy, SY_H2 + hfi + (lane & 3), tag);
        }
    }
    STAMP(9);
    f32x4 acc[2][9];
    const unsigned arel = ldsbase + (unsigned)((16 * (q4 >> 1) + 4 * (q4 & 1) + qq) * 288 + 8 * pp);
    const unsigned pdu = (unsigned)WS_P + (unsigned)(((tt * 4 + c4) * 8 + wave) * 8 + xj) * 9u * 1024u;
    const char* wob = (const char*)wo + c4 * 288;
    unsigned vw[4];
    auto wo_offsets = [&](bool last) {
        const int ln = fresh_lane();
#pragma unroll
        for (int i = 0; i < 4; ++i) {
            const int n = (mb + i) * 64 + ln, rs_ = n / 18, cc = n - rs_ * 18;
            const int u1 = rs_ >> 5, r1 = rs_ & 31, qx = 2 * (r1 >> 4) + ((r1 >> 2) & 1), tx = (r1 >> 3) & 1, qy = r1 & 3;
            const int f = 18 * (qx >> 1) + 4 * (qx & 1) + 8 * tx + qy;
            const int jx = 4 * tx + qy, g = 4 * (u1 - 1) + qx;
            const int row5 = (u1 == 0) ? 36 * 15 + f : 18 * (4 * g + (jx >> 1)) + 16 + (jx & 1);
            vw[i] = (unsigned)((last ? row5 : 36 * u1 + f) * 1152 + cc * 16 - 1024 * i);
        }
    };
    auto issue_wo = [&](int slot, int dst) { glds16x4(slot < 5 ? wob + (size_t)slot * 108 * 1152 : wob, vw, ldsbase + dst + mb * 1024); };
    {
        wait_vm<26>();
        BAR();
        constexpr int PF = 4;
        bf16x8 A[PF + 1];
        unsigned oflag = 0;
        constexpr int NS0 = 126, NST = 162;
        auto st_u = [](int idx) constexpr { return idx < NS0 ? idx / 9 : 14 + (idx - NS0) % 4; };
        auto st_dt = [](int idx) constexpr { return idx < NS0 ? idx % 9 : (idx - NS0) / 4; };
        unsigned cur = 0, curl = 0;
        auto rd = [&](auto IDX) {
            constexpr int idx = IDX, u = st_u(idx), dt = st_dt(idx);
            constexpr int ks = g2_ks(u), off = g2_place(ks / 3) + (ks % 3) * 32 * 288;
            if constexpr (idx < NS0) {
                if constexpr (dt == 0) cur = xor_add<0, off>(arel);
                return tr_frag_o(cur, dt * 32, dt * 32 + 8 * 288);
            } else {
                if constexpr (idx == NS0) { cur = xor_add<0, 0>(arel); curl = xor_add<0, WQ4>(arel); }
                if constexpr (off >= WQ4) return tr_frag_o(curl, off - WQ4 + dt * 32, off - WQ4 + dt * 32 + 8 * 288);
                else return tr_frag_o(cur, off + dt * 32, off + dt * 32 + 8 * 288);
            }
        };
        sfor<NST>([&](auto IDX) {
            constexpr int idx = IDX, u = st_u(idx), dt = st_dt(idx);
            if constexpr (idx < NS0 && dt == 0) {
                if constexpr (u == 0) STAMP(10);
                if constexpr (u == 3) { BBAR(); issue_wd(5, WQ0); }
                if constexpr (u == 6) {
                    wait_vm<12>();
                    sfor<6>([&](auto K) { pin(hf[6 + K][0]); pin(hf[6 + K][1]); });
                    BBAR();
                    if (tid0) flag_store(sy, SY_OF + tt * 64 + cgrp, tag);
                }
                if constexpr (u == 8) { if (wave == 0) gload4_sc1(oflag, (unsigned)(SY_OF + tt * 64 + (lane & 31)) * 4u, sy); }
                if constexpr (u == 10) {
                    wait_vm<8>();
                    if (wave == 0) {
                        wait_vm_u<0>(oflag);
                        if (!__all(oflag == tag)) wait_flags(sy, SY_OF + tt * 64 + (lane & 31), tag);
                        asm volatile("buffer_inv sc1" ::: "memory");
                    }
                    BBAR();
                }
                if constexpr (u == 12) {
                    if (wave == 0) wait_vm<1>(); else wait_vm<0>();
                    sfor<6>([&](auto K) { pin(hf[12 + K][0]); pin(hf[12 + K][1]); });
                    wo_offsets(false);
                }
                if constexpr (idx == 0) sfor<PF>([&](auto J) { A[J] = rd(J); });
                if constexpr (u < 6) wait_vm_dep2<ewait(u)>(hf[u][0], hf[u][1]);
            }
            if constexpr (idx == NS0) {
                if (wave == 0) wait_vm<0>();
                BBAR();
                const char* osrc = ws + WS_O + (size_t)((tt * 8 + xj) * 2) * 18 * 1024;
#pragma unroll
                for (int i = 0; i < 5; ++i) {
                    int m = wave + 8 * i; if (m >= 36) m -= 8;
                    glds16(osrc + m * 1024, l16, ldsbase + (m < 18 ? OTOP + m * 1024 : WQ3 + (m - 18) * 1024));
                }
                issue_wo(0, WQ1); issue_wo(1, WQ2);
            }
            if constexpr (idx + PF < NST) A[(idx + PF) % (PF + 1)] = rd(IC<idx + PF>{});
            SB();
            if constexpr (u == 0) {
                acc[0][dt] = MFMA16(A[idx % (PF + 1)], hf[u][0], ((f32x4){0.f, 0.f, 0.f, 0.f}));
                acc[1][dt] = MFMA16(A[idx % (PF + 1)], hf[u][1], ((f32x4){0.f, 0.f, 0.f, 0.f}));
            } else {
                acc[0][dt] = MFMA16(A[idx % (PF + 1)], hf[u][0], acc[0][dt]);
                acc[1][dt] = MFMA16(A[idx % (PF + 1)], hf[u][1], acc[1][dt]);
            }
            SB();
            if constexpr (idx < NS0 && dt == 2) {
                if constexpr (u == 0) issue_wd(3, WQ3);
                if constexpr (u == 1) { hload(IC<12>{}); hload(IC<13>{}); }
                if constexpr (u == 2) issue_wd(4, WQ4);
                if constexpr (u == 4) { hload(IC<14>{}); hload(IC<15>{}); }
                if constexpr (u == 5) { hload(IC<16>{}); hload(IC<17>{}); }
                SB();
            }
            if constexpr (idx >= NS0 && u == 16 && dt > 0) {
                constexpr int d = dt - 1;
                u32x4 w = {pk2(acc[0][d][0], acc[0][d][1]), pk2(acc[0][d][2], acc[0][d][3]), pk2(acc[1][d][0], acc[1][d][1]), pk2(acc[1][d][2], acc[1][d][3])};
                st16_sc1s(rs, l16, pdu + d * 1024, w);
                SB();
            }
        });
        STAMP(16);
        u32x4 w = {pk2(acc[0][8][0], acc[0][8][1]), pk2(acc[0][8][2], acc[0][8][3]), pk2(acc[1][8][0], acc[1][8][1]), pk2(acc[1][8][2], acc[1][8][3])};
        st16_sc1s(rs, l16, pdu + 8 * 1024, w);
    }
    {
        wait_vm<0>();
        BAR();
        if (tid0) flag_store(sy, SY_PF + (tt * 4 + c4) * 64 + xj, tag);
        STAMP(17);
        issue_wo(2, WQ0); issue_wo(3, WQ4);
        const int tok0 = tt * 256 + xj * 32;
        const unsigned psrc = (unsigned)WS_P + (unsigned)(((tt * 4 + c4) * 8 + xj) * 8) * 9u * 1024u + l16;
        auto fin = [&](auto NDC) {
            constexpr int ND = NDC;
            u32x2 xv[ND][2];
            u32x4 vy[ND][8];
            f32x4 sacc[ND][2];
            auto sgroup = [&](auto G) {
                constexpr int g = G;
                bf16x8 B0[3], B1[3], A[3][ND];
                auto rdk = [&](auto KK) {
                    constexpr int ks = 6 * g + KK, sl = ks / 3;
                    constexpr int place = sl == 0 ? WQ1 : sl == 1 ? WQ2 : sl == 2 ? WQ0 : sl == 3 ? WQ4 : sl == 4 ? WQ1 : WQ2;
                    B0[KK % 3] = __builtin_bit_cast(bf16x8, lds_rd128(ldsbase + OTOP + ks * 1024 + l16));
                    B1[KK % 3] = __builtin_bit_cast(bf16x8, lds_rd128(ldsbase + WQ3 + ks * 1024 + l16));
#pragma unroll
                    for (int k = 0; k < ND; ++k) A[KK % 3][k] = tr_frag_o(arel + place + (ks % 3) * 32 * 288 + ((k == 0) ? wave : 8) * 32, 0, 8 * 288);
                };
                rdk(IC<0>{}); rdk(IC<1>{});
                sfor<6>([&](auto KK) {
                    constexpr int kk = KK, ks = 6 * g + kk;
                    if constexpr (kk + 2 < 6) rdk(IC<kk + 2>{});
                    SB();
#pragma unroll
                    for (int k = 0; k < ND; ++k) {
                        if constexpr (ks == 0) { sacc[k][0] = MFMA16(A[kk % 3][k], B0[kk % 3], ((f32x4){0.f, 0.f, 0.f, 0.f})); sacc[k][1] = MFMA16(A[kk % 3][k], B1[kk % 3], ((f32x4){0.f, 0.f, 0.f, 0.f})); }
                        else { sacc[k][0] = MFMA16(A[kk % 3][k], B0[kk % 3], sacc[k][0]); sacc[k][1] = MFMA16(A[kk % 3][k], B1[kk % 3], sacc[k][1]); }
                    }
                    SB();
                });
            };
            sgroup(IC<0>{});
            BAR();
            glds16x4(wob + (size_t)4 * 108 * 1152, vw, ldsbase + WQ1 + mb * 1024);
            wo_offsets(true);
            issue_wo(5, WQ2);
#pragma unroll
            for (int k = 0; k < ND; ++k) {
                const int dt = (k == 0) ? wave : 8;
#pragma unroll
                for (int a = 0; a < 2; ++a)
                    xv[k][a] = *(const u32x2*)((const char*)x + (size_t)(tok0 + a * 16 + t16) * 1152 + (c4 * 144 + dt * 16 + 4 * q4) * 2);
            }
            wait_flags(sy, SY_PF + (tt * 4 + c4) * 64 + (lane & 7), tag);
            STAMP(18);
#pragma unroll
            for (int k = 0; k < ND; ++k) {
                const int dt = (k == 0) ? wave : 8;
#pragma unroll
                for (int sj = 0; sj < 8; ++sj) vy[k][sj] = __builtin_amdgcn_raw_buffer_load_b128(rs, psrc + (sj * 9 + dt) * 1024, 0, 16);
            }
            BBAR();
            sgroup(IC<1>{});
            sgroup(IC<2>{});
#pragma unroll
            for (int k = 0; k < ND; ++k) {
                const int dt = (k == 0) ? wave : 8;
                float y[8];
#pragma unroll
                for (int e = 0; e < 8; ++e) y[e] = 0.f;
#pragma unroll
                for (int sj = 0; sj < 8; ++sj)
#pragma unroll
                    for (int e = 0; e < 4; ++e) { y[2 * e] += lo2f(vy[k][sj][e]); y[2 * e + 1] += hi2f(vy[k][sj][e]); }
#pragma unroll
                for (int a = 0; a < 2; ++a) {
                    float ov[4];
#pragma unroll
                    for (int r = 0; r < 4; ++r) {
                        const float yy = rbf(y[4 * a + r]);
                        const float g = rbf(yy * __builtin_amdgcn_rcpf(1.f + __builtin_amdgcn_exp2f(-yy * 1.4426950408889634f)));
                        const float pr = rbf(rbf(sacc[k][a][r]) * g);
                        const unsigned xw = xv[k][a][r >> 1];
                        ov[r] = ((r & 1) ? hi2f(xw) : lo2f(xw)) + pr;
                    }
                    u32x2 w = {pk2(ov[0], ov[1]), pk2(ov[2], ov[3])};
                    *(u32x2*)((char*)out + (size_t)(tok0 + a * 16 + t16) * 1152 + (c4 * 144 + dt * 16 + 4 * q4) * 2) = w;
                }
            }
        };
        if (wave == 0) fin(IC<2>{}); else fin(IC<1>{});
        if (bid == 0 && wave == 7) {
            for (unsigned spins = 0; spins < kSpinCap; ++spins) {
                bool ok = true;
#pragma unroll
                for (int k = 0; k < 4; ++k) ok &= flag_load(sy, SY_START + lane + 64 * k) == tag;
                if (__all(ok)) break;
                __builtin_amdgcn_s_sleep(1);
            }
            if (lane == 0) flag_store(sy, SY_GEN, tag);
        }
    }
    }
    STAMP(19);
}

extern "C" void kernel_launch(void* const* d_in, const int* in_sizes, int n_in, void* d_out, int out_size, void* d_ws, size_t ws_size,
                              hipStream_t stream) {
    const u16* x = (const u16*)d_in[0];
    const u16* wqkv = (const u16*)d_in[1];
    const u16* wo = (const u16*)d_in[2];
    const u16* wg = (const u16*)d_in[4];
    const u16* wu = (const u16*)d_in[5];
    const u16* wd = (const u16*)d_in[6];
    (void)hipFuncSetAttribute((const void*)mega_kernel, hipFuncAttributeMaxDynamicSharedMemorySize, LDS_BYTES);
    mega_kernel<<<256, 512, LDS_BYTES, stream>>>(x, wqkv, wo, wg, wu, wd, (char*)d_ws, (u16*)d_out);
}
```

```cpp
#include <hip/hip_runtime.h>
#include <hip/hip_bf16.h>
#include <stdint.h>
#include <utility>

typedef unsigned short u16;
typedef __bf16 bf16x8 __attribute__((ext_vector_type(8)));
typedef __bf16 bf16x2 __attribute__((ext_vector_type(2)));
typedef float f32x16 __attribute__((ext_vector_type(16)));
typedef float f32x4 __attribute__((ext_vector_type(4)));
typedef float f32x2 __attribute__((ext_vector_type(2)));
typedef short s16x4 __attribute__((ext_vector_type(4)));
typedef short s16x8 __attribute__((ext_vector_type(8)));
typedef unsigned u32x4 __attribute__((ext_vector_type(4)));
typedef unsigned u32x2 __attribute__((ext_vector_type(2)));
#define LAS __attribute__((address_space(3)))
#define GAS __attribute__((address_space(1)))
#define DI __device__ __forceinline__

constexpr int S = 2048, D = 576, F = 4608;
constexpr size_t WS_SYNC = 0;
constexpr int SY_GEN = 0;
constexpr int SY_START = 64;
constexpr int SY_XF = 512;
constexpr int SY_QF = SY_XF + 8 * 64;
constexpr int SY_HF = SY_QF + 8 * 64;
constexpr int SY_OF = SY_HF + 64 * 64;
constexpr int SY_PF = SY_OF + 8 * 64;
constexpr int SY_H2 = SY_PF + 32 * 64;
constexpr int SY_END = SY_H2 + 64 * 64;
static_assert(SY_END * 4 <= 131072, "sync area");
constexpr size_t WS_H = 131072;
constexpr size_t WS_H_BYTES = (size_t)64 * 288 * 1024;
constexpr size_t WS_QKV = WS_H + WS_H_BYTES;
constexpr size_t WS_QKV_BYTES = (size_t)3 * 32 * 2048 * 48;
constexpr size_t WS_O = WS_QKV + WS_QKV_BYTES + 4096;
constexpr size_t WS_O_BYTES = (size_t)128 * 18 * 1024;
constexpr size_t WS_P = WS_O + WS_O_BYTES;
constexpr size_t WS_P_BYTES = (size_t)8 * 4 * 8 * 8 * 9 * 1024;
constexpr size_t WS_X = WS_P + WS_P_BYTES;
constexpr size_t WS_X_BYTES = (size_t)64 * 36 * 1024;
constexpr size_t WS_USED = WS_X + WS_X_BYTES;

DI float lo2f(unsigned v) { return __uint_as_float(v << 16); }
DI float hi2f(unsigned v) { return __uint_as_float(v & 0xffff0000u); }
DI unsigned pk2(float a, float b) { f32x2 t = {a, b}; return __builtin_bit_cast(unsigned, __builtin_convertvector(t, bf16x2)); }
DI float rbf(float f) { return lo2f(pk2(f, 0.f)); }
DI float dot2(unsigned a, unsigned b, float c) { return __builtin_amdgcn_fdot2_f32_bf16(__builtin_bit_cast(bf16x2, a), __builtin_bit_cast(bf16x2, b), c, false); }
DI float xhalf_max(float v) { auto r = __builtin_amdgcn_permlane32_swap(__float_as_uint(v), __float_as_uint(v), false, false); return fmaxf(__uint_as_float(r[0]), __uint_as_float(r[1])); }
DI float lo_bcast(float v) { auto r = __builtin_amdgcn_permlane32_swap(__float_as_uint(v), __float_as_uint(v), false, false); return __uint_as_float(r[0]); }

DI void glds16(const void* sbase, unsigned voff, unsigned lds_dst) {
    asm volatile("s_mov_b32 m0, %2\n\ts_nop 0\n\tglobal_load_lds_dwordx4 %0, %1" ::"v"(voff), "s"(sbase), "s"(lds_dst) : "memory");
}
DI void glds16x3(const void* sbase, unsigned v0, unsigned v1, unsigned v2, unsigned lds_dst) {
    asm volatile("s_mov_b32 m0, %4\n\ts_nop 0\n\tglobal_load_lds_dwordx4 %0, %3\n\tglobal_load_lds_dwordx4 %1, %3 offset:1024\n\tglobal_load_lds_dwordx4 %2, %3 offset:2048"
                 ::"v"(v0), "v"(v1), "v"(v2), "s"(sbase), "s"(lds_dst) : "memory");
}
DI void glds16x4(const void* sbase, const unsigned (&v)[4], unsigned lds_dst) {
    asm volatile("s_mov_b32 m0, %5\n\ts_nop 0\n\tglobal_load_lds_dwordx4 %0, %4\n\tglobal_load_lds_dwordx4 %1, %4 offset:1024\n\tglobal_load_lds_dwordx4 %2, %4 offset:2048\n\tglobal_load_lds_dwordx4 %3, %4 offset:3072"
                 ::"v"(v[0]), "v"(v[1]), "v"(v[2]), "v"(v[3]), "s"(sbase), "s"(lds_dst) : "memory");
}
template <int OFF> DI void gload16_sc1(bf16x8& dst, unsigned voff, const char* sbase) {
    asm volatile("global_load_dwordx4 %0, %1, %2 offset:%3 sc1" : "=v"(dst) : "v"(voff), "s"(sbase), "i"(OFF) : "memory");
}
DI void gload4_plain(unsigned& dst, unsigned voff, const void* sbase) {
    asm volatile("global_load_dword %0, %1, %2" : "+v"(dst) : "v"(voff), "s"(sbase) : "memory");
}
DI void gload4_sc1(unsigned& dst, unsigned voff, const void* sbase) {
    asm volatile("global_load_dword %0, %1, %2 sc1" : "=v"(dst) : "v"(voff), "s"(sbase) : "memory");
}
template <int N> DI void wait_vm_dep(bf16x8& v) { asm volatile("s_waitcnt vmcnt(%1)" : "+v"(v) : "i"(N)); }
template <int N> DI void wait_vm_dep2(bf16x8& v, bf16x8& w) { asm volatile("s_waitcnt vmcnt(%2)" : "+v"(v), "+v"(w) : "i"(N)); }
template <int N> DI void wait_vm_u(unsigned& v) { asm volatile("s_waitcnt vmcnt(%1)" : "+v"(v) : "i"(N)); }
template <int N> DI void wait_vm() { asm volatile("s_waitcnt vmcnt(%0)" ::"i"(N) : "memory"); }
#define WAIT_LGKM0() asm volatile("s_waitcnt lgkmcnt(0)" ::: "memory")
#define BAR() do { asm volatile("s_waitcnt lgkmcnt(0)" ::: "memory"); __builtin_amdgcn_s_barrier(); asm volatile("" ::: "memory"); } while (0)
#define BBAR() do { asm volatile("" ::: "memory"); __builtin_amdgcn_s_barrier(); asm volatile("" ::: "memory"); } while (0)
#define SB() __builtin_amdgcn_sched_barrier(0)
template <int... Is, class Fn> DI void sfor_impl(std::integer_sequence<int, Is...>, Fn&& f) { (f(std::integral_constant<int, Is>{}), ...); }
template <int N, class Fn> DI void sfor(Fn&& f) { sfor_impl(std::make_integer_sequence<int, N>{}, f); }
template <int V> using IC = std::integral_constant<int, V>;
DI void pin(bf16x8& v) { asm volatile("" : "+v"(v)); }
DI float glu_a(float g) { asm volatile("v_mul_f32 %0, 0xbfb8aa3b, %0\n\tv_exp_f32 %0, %0" : "+v"(g)); return g; }
DI float glu_b(float e, float u) { asm volatile("v_add_f32 %0, 1.0, %0\n\tv_rcp_f32 %0, %0\n\ts_nop 0\n\tv_mul_f32 %0, %0, %1" : "+v"(e) : "v"(u)); return e; }
template <int X, int ADD> DI unsigned xor_add(unsigned base) {
    unsigned r; asm volatile("v_xor_b32 %0, %2, %1\n\tv_add_u32 %0, %3, %0" : "=&v"(r) : "v"(base), "i"(X), "i"(ADD)); return r;
}
DI unsigned opaque(unsigned v) { asm volatile("" : "+v"(v)); return v; }
DI int fresh_lane() { int l; asm volatile("v_mbcnt_lo_u32_b32 %0, -1, 0\n\tv_mbcnt_hi_u32_b32 %0, -1, %0" : "=v"(l)); return l; }
DI bf16x8 tr_frag_o(unsigned base, int off0, int off1) {
    s16x4 lo = __builtin_amdgcn_ds_read_tr16_b64_v4i16((LAS s16x4*)((LAS char*)(size_t)base + off0));
    s16x4 hi = __builtin_amdgcn_ds_read_tr16_b64_v4i16((LAS s16x4*)((LAS char*)(size_t)base + off1));
    s16x8 f = __builtin_shufflevector(lo, hi, 0, 1, 2, 3, 4, 5, 6, 7);
    return __builtin_bit_cast(bf16x8, f);
}
DI u32x4 lds_rd128(unsigned a) { return *(LAS u32x4*)(size_t)a; }
DI unsigned lds_rd32(unsigned a) { return *(LAS unsigned*)(size_t)a; }
#define MFMA32(a, b, c) __builtin_amdgcn_mfma_f32_32x32x16_bf16((a), (b), (c), 0, 0, 0)
#define MFMA16(a, b, c) __builtin_amdgcn_mfma_f32_16x16x32_bf16((a), (b), (c), 0, 0, 0)

typedef __amdgpu_buffer_rsrc_t rsrc_t;
DI void st16_sc1(rsrc_t r, unsigned off, u32x4 v) { __builtin_amdgcn_raw_buffer_store_b128(v, r, off, 0, 16); }
DI void st16_sc1s(rsrc_t r, unsigned voff, unsigned soff, u32x4 v) {
    asm volatile("buffer_store_dwordx4 %0, %1, %2, %3 offen sc1\n\ts_nop 1" ::"v"(v), "v"(voff), "s"(r), "s"(soff) : "memory");
}
DI void st4_sc1(rsrc_t r, unsigned off, unsigned v) { __builtin_amdgcn_raw_buffer_store_b32(v, r, off, 0, 16); }
DI void flag_store(unsigned* sy, int idx, unsigned tag) { __hip_atomic_store((GAS unsigned*)sy + idx, tag, __ATOMIC_RELAXED, __HIP_MEMORY_SCOPE_AGENT); }
DI unsigned flag_load(const unsigned* sy, int idx) { return __hip_atomic_load((GAS unsigned*)sy + idx, __ATOMIC_RELAXED, __HIP_MEMORY_SCOPE_AGENT); }
constexpr unsigned kSpinCap = 1u << 20;
DI void wait_flags(const unsigned* sy, int idx, unsigned tag) {
    for (unsigned spins = 0; spins < kSpinCap; ++spins) {
        const unsigned v = flag_load(sy, idx);
        if (__all(v == tag)) break;
        __builtin_amdgcn_s_sleep(1);
    }
    asm volatile("" ::: "memory");
}

#define STAMP(i) do {} while (0)

template <int CTRL, int RMASK> DI float dpp_add(float v) {
    return v + __int_as_float(__builtin_amdgcn_update_dpp(0, __float_as_int(v), CTRL, RMASK, 0xf, true));
}
DI float wave_sum(float v) {
    v = dpp_add<0x111, 0xf>(v); v = dpp_add<0x112, 0xf>(v); v = dpp_add<0x114, 0xf>(v); v = dpp_add<0x118, 0xf>(v);
    v = dpp_add<0x142, 0xa>(v); v = dpp_add<0x143, 0xc>(v);
    return __int_as_float(__builtin_amdgcn_readlane(__float_as_int(v), 63));
}

constexpr int SLOT = 24576;
constexpr int SPARE = SLOT * 6;
constexpr int LDS_BYTES = 163840;
constexpr int KPOS = 4 * SLOT, VPOS = 5 * SLOT;
constexpr int WSL = 27648;
constexpr int WQ0 = 0, WQ1 = WSL, WQ2 = 2 * WSL, WQ3 = 3 * WSL, WQ4 = 4 * WSL, OTOP = 5 * WSL;
static_assert(WQ3 <= KPOS && OTOP + 18 * 1024 <= LDS_BYTES, "lds");

struct Ctx {
    const char* wq; const char* wgu;
    unsigned vq0, vq1, vq2, vg0, vg1, vg2;
    unsigned ldsbase;
    int wave; int P0;
    const char* wa; const char* wb; unsigned la, lb;
};
DI void issue_q(const Ctx& c, int chunk, int pos) { glds16x3(c.wq + (size_t)(chunk * 192) * 3456, c.vq0, c.vq1, c.vq2, c.ldsbase + pos * SLOT + c.wave * 3072); }
DI void issue_gu(const Ctx& c, int panel, int chunk, int pos) {
    glds16x3(c.wgu + (size_t)(chunk * 96) * 9216 + (size_t)(c.P0 + panel) * 128, c.vg0, c.vg1, c.vg2, c.ldsbase + pos * SLOT + c.wave * 3072);
}

constexpr int PF1 = 3;
DI void issue_slot(const Ctx& c, int m) {
    if (m < 6) issue_gu(c, 1, m, m % 6);
    else if (m < 9) issue_q(c, m - 6, m % 6);
    else if (m < 15) issue_gu(c, 0, m - 9, m % 6);
    else issue_gu(c, 2, m - 15, m % 6);
}

struct Hand {
    unsigned* sy; unsigned tag; rsrc_t rs; int tt, j, c4, cgrp;
    const char* kb; const char* vb; const char* qb;
};

constexpr int NSTEPS = 396;
struct Step { short slot, ks, t, set, tau, first, fmt; };
struct Sched {
    Step st[NSTEPS];
    short fill[NSTEPS + 100][3];
    short nrows;
    short qn;
    short qent[24][5];
    short qlast;
};
template <int C4> constexpr Sched make_sched() {
    Sched S{};
    const int NTa = 4 - C4, NTc = 1 + C4;
    int setsA[4] = {2, 3, 0, 1};
    int setsC[4] = {0, 0, 0, 0};
    {
        int k = 0;
        for (int s = 0; s < 4; ++s) { int order[4] = {3, 0, 1, 2}; int cand = order[s]; bool used = false; for (int t = 0; t < NTa; ++t) used |= (setsA[t] == cand); if (!used && k < NTc - 1) setsC[k++] = cand; }
        setsC[NTc - 1] = setsA[0];
    }
    int n = 0, lastStep[11] = {};
    for (int slot = 0; slot < 21; ++slot) {
        int pass = slot < 6 ? 0 : slot < 9 ? 1 : slot < 15 ? 2 : 3;
        int i = pass == 0 ? slot : pass == 1 ? slot - 6 : pass == 2 ? slot - 9 : slot - 15;
        int nt = pass == 0 ? 4 : pass == 1 ? 2 : pass == 2 ? NTa : NTc;
        int kps = pass == 1 ? 12 : 6;
        int nsl = pass == 1 ? 3 : 6;
        int tau0 = pass == 0 ? 0 : pass == 1 ? 4 : pass == 2 ? 6 : 6 + NTa;
        bool tmajor = (i == nsl - 1) || (i == 0 && pass > 0);
        for (int k = 0; k < nt * kps; ++k) {
            int t = tmajor ? k / kps : k % nt, s = tmajor ? k % kps : k / nt;
            Step e{};
            e.slot = slot; e.ks = i * kps + s; e.t = t; e.tau = tau0 + t; e.fmt = pass == 1; e.first = (e.ks == 0);
            e.set = pass == 0 ? t : pass == 1 ? t : pass == 2 ? setsA[t] : setsC[t];
            S.st[n] = e; lastStep[e.tau] = n; ++n;
        }
    }
    const int delta = C4 == 0 ? 0 : C4 == 1 ? 6 : C4 == 2 ? 4 : 2;
    int qn = 0;
    for (int sl = 0; sl < 3; ++sl)
        for (int t = 0; t < 2; ++t)
            for (int r = 0; r < 16; r += 2) {
                int m = 0, d0 = 0;
                for (int hh = 1; hh >= 0; --hh) { int nn = 32 * t + (r & 3) + 8 * (r >> 2) + 4 * hh - delta; if (nn >= 0 && nn < 54 && nn / 18 == sl) { m |= 1 << hh; d0 = nn % 18 - 4 * hh; } }
                if (m) { S.qent[qn][0] = t; S.qent[qn][1] = r; S.qent[qn][2] = sl; S.qent[qn][3] = m; S.qent[qn][4] = d0; ++qn; }
            }
    S.qn = qn;
    int freeg = 0;
    auto put = [&](int g, int code) { for (int k = 0; k < 3; ++k) if (S.fill[g][k] == 0) { S.fill[g][k] = code; return; } S.fill[g][0] = -1; };
    for (int tau = 0; tau < 11; ++tau) {
        if (tau == 4) continue;
        int ready = lastStep[tau] + 3;
        int g = ready > freeg ? ready : freeg;
        int dl = 100000;
        for (int k = lastStep[tau] + 1; k < NSTEPS; ++k) if (S.st[k].first && (S.st[k].set == S.st[lastStep[tau]].set || (tau == 5 && S.st[k].set == S.st[lastStep[4]].set))) { dl = k; break; }
        int ops[40] = {}, cnt = 0;
        if (tau == 5) {
            for (int sl = 0; sl < 3; ++sl) {
                for (int e = 0; e < qn; ++e) if (S.qent[e][2] == sl) ops[cnt++] = 2000 + e;
                ops[cnt++] = 2100 + sl; ops[cnt++] = 2110 + sl;
            }
        } else {
            const int b = 1000 + tau * 32;
            ops[cnt++] = b + 0;
            for (int j = 0; j < 7; ++j) { ops[cnt++] = b + j + 1; ops[cnt++] = b + 8 + j; }
            ops[cnt++] = b + 15; ops[cnt++] = b + 16; ops[cnt++] = b + 17;
        }
        int avail = dl - g, dbl = cnt > avail ? cnt - avail : 0;
        if (tau == 5 && dbl < cnt / 2) dbl = cnt / 2;
        int k = 0;
        while (k < cnt) { put(g, ops[k++]); if (dbl > 0 && k < cnt) { put(g, ops[k++]); --dbl; } ++g; }
        if (tau == 5) { for (int q = 0; q < NSTEPS + 100; ++q) for (int z = 0; z < 3; ++z) if (S.fill[q][z] == 2112) S.qlast = q; }
        freeg = g;
    }
    S.nrows = freeg;
    return S;
}
template <int C4> struct SchedOf { static constexpr Sched SC = make_sched<C4>(); };
template <int C4> constexpr int set_of(int tau) { for (int g = 0; g < NSTEPS; ++g) if (SchedOf<C4>::SC.st[g].tau == tau) return SchedOf<C4>::SC.st[g].set; return 0; }
constexpr int xpos(int s) { return s < 6 ? -1 : 4 * ((s - 6) / 3) + 2; }
constexpr int dpos(int d) {
    return d == 2 ? -1 : d == 3 ? 4 * 3 + 1 : d == 4 ? 4 * 9 + 1 : d == 5 ? 4 * 15 + 1 : d == 6 ? 4 * 21 + 1 : d == 7 ? 4 * 27 + 1 : d == 8 ? 24 * 5 + 1
         : d == 9 || d == 10 ? 24 * 6 + 1 : d == 11 || d == 12 ? 24 * 7 + 1 : 24 * (d - 5) + 1;
}
constexpr bool d_after(int e, int d) { return dpos(e) > dpos(d) || (dpos(e) == dpos(d) && e > d); }
constexpr int younger_than_x(int s, int now) {
    int n = 0;
    for (int t = s + 1; t < 36; ++t) if (xpos(t) < now) ++n;
    for (int d = 2; d <= 20; ++d) if (dpos(d) < now && (dpos(d) > xpos(s) || (dpos(d) == xpos(s) && d == 2))) n += 3;
    return n;
}
constexpr int younger_than_d(int d, int now) {
    int n = 0;
    for (int t = 0; t < 36; ++t) if (xpos(t) < now && xpos(t) > dpos(d)) ++n;
    for (int e = 2; e <= 20; ++e) if (dpos(e) < now && d_after(e, d)) n += 3;
    return n;
}

template <int C4>
DI void gemm1(const Ctx& c, const Hand& hd, char* ws, int tile32) {
#define S (SchedOf<C4>::SC)
    constexpr int NTa = 4 - C4;
    constexpr int NK = (C4 <= 1) ? 17 : (C4 == 2 ? 18 : 19), NV = (C4 <= 2) ? 18 : 19;
    constexpr int NCHK = C4 == 0 ? 14 : C4 == 1 ? 15 : C4 == 2 ? 17 : 18;
    constexpr int NQF = S.st[S.qlast < NSTEPS ? S.qlast : NSTEPS - 1].slot + 2;
    static_assert(NQF <= 13 && NQF >= 9, "qkv publish");
    unsigned Lgu, Lq = 0, l16;
    {
        const int lane = fresh_lane();
        const int hh = lane >> 5, blk = (lane >> 4) & 1, q = (lane & 15) >> 2, pp = lane & 3;
        Lgu = c.ldsbase + (unsigned)(blk * 12288 + (8 * hh + q) * 128 + 8 * pp + 32 * (((q >> 1) & 1) | (blk << 1)));
        l16 = lane * 16;
    }
    const unsigned hbs = (unsigned)WS_H + ((unsigned)tile32 * 288u + (unsigned)c.P0 * 4u) * 1024u;
    const unsigned stg = c.ldsbase + SPARE + c.wave * 2048;
    const unsigned qds = (unsigned)WS_QKV + (unsigned)((hd.cgrp * 3) * 2048 + tile32 * 32) * 48u;
    unsigned stw = 0;
    const char* xs = ws + WS_X + (size_t)tile32 * 36 * 1024;
    bf16x8 xf[36];
    f32x16 acc[4];
    unsigned cur[4];
    bf16x8 A[PF1 + 1];
    unsigned qflag = 0, pl0 = 0, pl1 = 0, warm0 = 0, warm1 = 0;
    sfor<6>([&](auto SI) { constexpr int s = SI; gload16_sc1<(s & 3) * 1024>(xf[s], l16, xs + (s >> 2) * 4096); });
    issue_slot(c, 2);
    auto rd = [&](auto IDX) {
        constexpr Step e = S.st[IDX];
        constexpr int kl = e.fmt ? e.ks % 12 : e.ks % 6, pos = e.slot % 6;
        constexpr int col = e.fmt ? e.t : (e.slot >= 9 && e.slot < 15 ? C4 + e.t : e.t);
        if constexpr (e.fmt && e.ks == 0 && e.t == 0) {
            const int lane = fresh_lane();
            const int hh = lane >> 5, blk = (lane >> 4) & 1, q = (lane & 15) >> 2, pp = lane & 3;
            Lq = c.ldsbase + (unsigned)((8 * hh + q) * 128 + 8 * pp + 32 * (blk ^ (((q >> 1) & 1) << 1)));
        }
        if constexpr (kl == 0) cur[e.t] = e.fmt ? xor_add<(col << 6), pos * SLOT>(Lq) : xor_add<(col << 5), pos * SLOT>(Lgu);
        return tr_frag_o(cur[e.t], kl * 2048, kl * 2048 + 512);
    };
    auto filler = [&](auto CODE) {
        constexpr int code = CODE;
        if constexpr (code >= 1000 && code < 2000) {
            constexpr int tau = (code - 1000) / 32, j = (code - 1000) % 32;
            constexpr int realset = set_of<C4>(tau);
            constexpr int fgo = tau < 4 ? 4 + tau : (tau < 6 + NTa ? C4 + tau - 6 : 8 + tau - 6 - NTa);
            if constexpr (j < 8) acc[realset][j] = glu_a(acc[realset][j]);
            else if constexpr (j < 16) acc[realset][j - 8] = glu_b(acc[realset][j - 8], acc[realset][j]);
            else if constexpr (j == 16) {
                pl0 = pk2(acc[realset][0], acc[realset][1]);
                pl1 = pk2(acc[realset][2], acc[realset][3]);
            } else {
                u32x4 w = {pl0, pl1, pk2(acc[realset][4], acc[realset][5]), pk2(acc[realset][6], acc[realset][7])};
                st16_sc1s(hd.rs, l16, hbs + fgo * 1024, w);
            }
        } else if constexpr (code >= 2000 && code < 2100) {
            constexpr int e = code - 2000, t = S.qent[e][0], r = S.qent[e][1], m = S.qent[e][3], d0 = S.qent[e][4];
            if constexpr (e == 0) { const int ln = fresh_lane(); stw = opaque(stg + (ln & 31) * 48 + ((ln >> 5) << 3)); }
            const float qs = (hd.cgrp * 3 + S.qent[e][2] < 32) ? 0.23570226039551584f * 1.4426950408889634f : 1.f;
            const unsigned v = pk2(acc[t][r] * qs, acc[t][r + 1] * qs);
            if constexpr (m == 3) *(LAS unsigned*)(size_t)(stw + 2 * d0) = v;
            else if ((int)((stw >> 3) & 1) == (m >> 1)) *(LAS unsigned*)(size_t)(stw + 2 * d0) = v;
        } else if constexpr (code >= 2100 && code < 2110) {
            constexpr int sl = code - 2100;
            if (((stw >> 3) & 1) == 0) *(LAS unsigned*)(size_t)(stw + 36) = (hd.cgrp * 3 + sl >= 64) ? 0x00003F80u : 0u;
            else *(LAS u32x2*)(size_t)(stw + 32) = (u32x2){0u, 0u};
        } else if constexpr (code >= 2110) {
            constexpr int sl = code - 2110;
            const u32x4 w0 = lds_rd128(stg + l16);
            st16_sc1s(hd.rs, l16, qds + (unsigned)sl * 2048u * 48u, w0);
            if (fresh_lane() < 32) { const u32x4 w1 = lds_rd128(stg + 1024 + l16); st16_sc1s(hd.rs, l16, qds + (unsigned)sl * 2048u * 48u + 1024, w1); }
        }
    };
    sfor<NSTEPS>([&](auto IDX) {
        constexpr int idx = IDX;
        constexpr Step e = S.st[idx];
        constexpr bool top = (idx == 0) || (S.st[idx > 0 ? idx - 1 : 0].slot != e.slot);
        if constexpr (top) {
            constexpr int n = e.slot;
            if constexpr (n == 5) {
                wait_vm<(younger_than_x(35, 120) < younger_than_d(6, 120) ? younger_than_x(35, 120) : younger_than_d(6, 120))>();
                pin(xf[30]); pin(xf[31]); pin(xf[32]); pin(xf[33]); pin(xf[34]); pin(xf[35]);
            } else if constexpr (n == NQF) wait_vm<3>();
            else if constexpr (n >= 1 && n <= 16) wait_vm<younger_than_d(n + 1, 24 * n)>();
            else if constexpr (n == 17) wait_vm<6>();
            else if constexpr (n == 18) wait_vm<3 + (NK <= 17 ? 3 : 0)>();
            else if constexpr (n == 19) wait_vm<(NK <= 18 ? 3 : 0) + (NV <= 18 ? 5 : 0)>();
            if constexpr (n == 7) asm volatile("" ::"v"(warm0), "v"(warm1));
            if constexpr (n == NCHK) {
                if (c.wave == 0) {
                    wait_vm_u<(n + 4 <= 20 ? 3 : 0)>(qflag);
                    if (!__all(qflag == hd.tag)) { const int ln = fresh_lane(); wait_flags(hd.sy, SY_QF + ((ln >= 32 && hd.tt > 0) ? hd.tt - 1 : hd.tt) * 64 + (ln & 31), hd.tag); }
                    asm volatile("buffer_inv sc1" ::: "memory");
                }
            }
            if constexpr (n == NK) { if (c.wave == 0) wait_vm<(NCHK + 5 <= 20 ? 3 * (20 - (NCHK + 5) + 1) : 0)>(); }
            if constexpr (n > 0) BBAR();
            if constexpr (n == NQF) { if (c.wave == 0 && fresh_lane() == 0) flag_store(hd.sy, SY_QF + hd.tt * 64 + hd.cgrp, hd.tag); }
            if constexpr (n == NCHK - 1) { if (c.wave == 0) { const int lane = fresh_lane(); gload4_sc1(qflag, (unsigned)(SY_QF + ((lane >= 32 && hd.tt > 0) ? hd.tt - 1 : hd.tt) * 64 + (lane & 31)) * 4u, hd.sy); } }
            sfor<19>([&](auto DD) { constexpr int d = DD + 2; if constexpr (dpos(d) == 24 * n + 1 && n >= 5) issue_slot(c, d); });
            if constexpr (n == NK || n == NV) {
#pragma unroll
                for (int kv = 0; kv < 2; ++kv) {
                    if ((kv == 0 && n == NK) || (kv == 1 && n == NV)) {
#pragma unroll
                        for (int k = 0; k < 3; ++k) {
                            const int m = c.wave * 3 + k;
                            int off = (hd.tt * 256 - 256) * 48 + m * 1024;
                            if (off < 0) off = m * 1024;
                            glds16((kv == 0 ? hd.kb : hd.vb) + off, l16, c.ldsbase + (kv == 0 ? KPOS : VPOS) + m * 1024);
                        }
                    }
                }
            }
            if constexpr (n == NV) {
#pragma unroll
                for (int k = 0; k < 2; ++k) {
                    int m = c.wave * 2 + k; if (m > 11) m = 11;
                    glds16(hd.qb + m * 1024, l16, c.ldsbase + SPARE + m * 1024);
                }
            }
            if constexpr (idx == 0) sfor<PF1>([&](auto J) { A[J] = rd(J); });
        }
        if constexpr (idx + PF1 < NSTEPS) A[(idx + PF1) % (PF1 + 1)] = rd(IC<idx + PF1>{});
        if constexpr (e.slot < 5 && e.t == 0) {
            sfor<6>([&](auto DD) { constexpr int d = DD + 3; if constexpr (d <= 7 && dpos(d) == 4 * e.ks + 1) issue_slot(c, d); });
            sfor<3>([&](auto JJ) { constexpr int sx = 6 + 3 * e.ks + JJ; if constexpr (sx < 36) gload16_sc1<(sx & 3) * 1024>(xf[sx], l16, xs + (sx >> 2) * 4096); });
            if constexpr (e.ks == 10) {
                const unsigned o = (unsigned)c.wave * 8192u + (unsigned)fresh_lane() * 128u;
                if (blockIdx.x < 193) gload4_plain(warm0, o < c.la ? o : c.la, c.wa);
                if (blockIdx.x < 92) gload4_plain(warm1, o < c.lb ? o : c.lb, c.wb);
            }
            wait_vm_dep<younger_than_x(e.ks, 4 * e.ks + 3)>(xf[e.ks]);
        }
        SB();
        if constexpr (e.first) {
            f32x16 z;
#pragma unroll
            for (int r = 0; r < 16; ++r) z[r] = 0.f;
            acc[e.set] = MFMA32(A[idx % (PF1 + 1)], xf[e.ks], z);
        } else acc[e.set] = MFMA32(A[idx % (PF1 + 1)], xf[e.ks], acc[e.set]);
        SB();
        if constexpr (S.fill[idx][0] != 0) {
            filler(IC<S.fill[idx][0]>{});
            if constexpr (S.fill[idx][1] != 0) filler(IC<S.fill[idx][1]>{});
            if constexpr (S.fill[idx][2] != 0) filler(IC<S.fill[idx][2]>{});
            SB();
        }
    });
    sfor<S.nrows - NSTEPS>([&](auto R) {
        constexpr int g = NSTEPS + R;
        if constexpr (S.fill[g][0] != 0) filler(IC<S.fill[g][0]>{});
        if constexpr (S.fill[g][1] != 0) filler(IC<S.fill[g][1]>{});
        if constexpr (S.fill[g][2] != 0) filler(IC<S.fill[g][2]>{});
    });
}
#undef S

constexpr int g2_ks(int u) { constexpr int o[18] = {0, 1, 2, 3, 5, 6, 7, 9, 10, 11, 14, 15, 4, 8, 12, 13, 16, 17}; return o[u]; }
constexpr int g2_place(int sl) { return sl == 0 ? WQ0 : sl == 1 ? WQ1 : sl == 2 ? WQ2 : sl == 3 ? WQ3 : sl == 4 ? WQ4 : WQ0; }
constexpr int ewait(int u) { constexpr int c[8] = {0, 4, 8, 16, 16, 20, 24, 24}; return 2 * (11 - u) + 2 + c[u]; }

__global__ __launch_bounds__(512, 2) void mega_kernel(const u16* __restrict__ x, const u16* __restrict__ wqkv, const u16* __restrict__ wo,
                                                      const u16* __restrict__ wg, const u16* __restrict__ wu, const u16* __restrict__ wd,
                                                      char* __restrict__ ws, u16* __restrict__ out) {
    extern __shared__ __attribute__((aligned(16))) char smem[];
    const int tid = threadIdx.x;
    const int wave = __builtin_amdgcn_readfirstlane(tid >> 6);
    const int bid = blockIdx.x, xj = bid & 7, loc = bid >> 3;
    const int c4 = loc & 3, tt = loc >> 2, cgrp = xj * 4 + c4;
    const unsigned ldsbase = (unsigned)(size_t)(LAS char*)smem;
    unsigned* sy = (unsigned*)(ws + WS_SYNC);
    const rsrc_t rs = __builtin_amdgcn_make_buffer_rsrc(ws, 0, (unsigned)WS_USED, 0x00020000);
    unsigned tag;
    Ctx c;

    STAMP(0);
    {
        const int lane = tid & 63;
        const int xrow = bid * 8 + wave;
        const char* xr = (const char*)x + (size_t)xrow * 1152;
        u32x4 xv0 = *(const u32x4*)(xr + lane * 16);
        u32x4 xv1 = *(const u32x4*)(xr + 1024 + (lane & 7) * 16);
        const unsigned gen = flag_load(sy, SY_GEN);
        const int qa0 = (108 * cgrp) & ~15;
        c.wq = (const char*)wqkv + qa0; c.wgu = (const char*)((wave >> 2) ? wu : wg);
        c.ldsbase = ldsbase; c.wave = wave; c.P0 = (9 * cgrp) >> 2;
        {
            const int lrow = lane >> 3, c16 = lane & 7, bsel = wave >> 2;
            const int swgu = ((lrow >> 1) & 1) | (bsel << 1);
            const int chgu = (((c16 >> 1) ^ swgu) << 1) | (c16 & 1);
            c.vg0 = (unsigned)((24 * (wave & 3) + lrow) * 9216 + chgu * 16); c.vg1 = c.vg0 + 8 * 9216 - 1024; c.vg2 = c.vg0 + 16 * 9216 - 2048;
            const int swq = ((lrow >> 1) & 1) << 1;
            int chq = (((c16 >> 1) ^ swq) << 1) | (c16 & 1);
            if (cgrp == 31 && chq == 7) chq = 6;
            c.vq0 = (unsigned)((24 * wave + lrow) * 3456 + chq * 16); c.vq1 = c.vq0 + 8 * 3456 - 1024; c.vq2 = c.vq0 + 16 * 3456 - 2048;
        }
        tag = (unsigned)__builtin_amdgcn_readfirstlane((int)(gen * 0x9E3779B1u + 0x7F4A7C15u));
        if (bid < 81) { c.wa = (const char*)wg + (size_t)bid * 65536; c.la = 65536 - 128; }
        else if (bid < 162) { c.wa = (const char*)wu + (size_t)(bid - 81) * 65536; c.la = 65536 - 128; }
        else { const int k = (bid - 162) % 31; c.wa = (const char*)wqkv + (size_t)k * 65536; c.la = (k == 30) ? 1990656 - 30 * 65536 - 128 : 65536 - 128; }
        if (bid < 81) { c.wb = (const char*)wd + (size_t)bid * 65536; c.lb = 65536 - 128; }
        else { const int k = (bid - 81) % 11; c.wb = (const char*)wo + (size_t)k * 65536; c.lb = (k == 10) ? 663552 - 10 * 65536 - 128 : 65536 - 128; }
        float s1 = 0.f;
        if (lane >= 8) xv1 = (u32x4){0u, 0u, 0u, 0u};
#pragma unroll
        for (int k = 0; k < 4; ++k) { s1 = dot2(xv0[k], 0x3f803f80u, s1); s1 = dot2(xv1[k], 0x3f803f80u, s1); }
        const float meanf = wave_sum(s1) * (1.f / 576.f);
        float s2 = 0.f;
#pragma unroll
        for (int k = 0; k < 4; ++k) {
            float a = lo2f(xv0[k]) - meanf, b = hi2f(xv0[k]) - meanf; s2 += a * a + b * b;
            if (lane < 8) { a = lo2f(xv1[k]) - meanf; b = hi2f(xv1[k]) - meanf; s2 += a * a + b * b; }
        }
        const float var = rbf(wave_sum(s2) * (1.f / 576.f));
        const float mean = rbf(meanf);
        const float rden = 1.f / rbf(sqrtf(rbf(var + 1e-5f)));
        u32x4 o0, o1;
#pragma unroll
        for (int k = 0; k < 4; ++k) {
            o0[k] = pk2(rbf(lo2f(xv0[k]) - mean) * rden, rbf(hi2f(xv0[k]) - mean) * rden);
            o1[k] = pk2(rbf(lo2f(xv1[k]) - mean) * rden, rbf(hi2f(xv1[k]) - mean) * rden);
        }
        *(LAS u32x4*)(size_t)(ldsbase + SPARE + (lane * 8 + wave) * 16) = o0;
        if (lane < 8) *(LAS u32x4*)(size_t)(ldsbase + SPARE + ((64 + lane) * 8 + wave) * 16) = o1;
        BAR();
        const unsigned xd = (unsigned)WS_X + (unsigned)(bid >> 2) * 36u * 1024u;
        const int t0 = (bid & 3) * 8;
        {
            const int i = tid, cc = i >> 3, tk = i & 7;
            st16_sc1(rs, xd + ((cc >> 1) * 64 + (cc & 1) * 32 + t0 + tk) * 16, lds_rd128(ldsbase + SPARE + i * 16));
        }
        {
            const int i = 512 + lane, cc = i >> 3, tk = i & 7;
            st16_sc1(rs, wave == 0 ? xd + ((cc >> 1) * 64 + (cc & 1) * 32 + t0 + tk) * 16 : 0xFFFFFF00u, lds_rd128(ldsbase + SPARE + i * 16));
        }
        issue_slot(c, 0); issue_slot(c, 1);
        wait_vm<6>();
        BBAR();
        if (tid == 0) { flag_store(sy, SY_XF + (bid >> 5) * 64 + (bid & 31), tag); flag_store(sy, SY_START + bid, tag); }
    }
    STAMP(1);
    Hand hd;
    hd.sy = sy; hd.tag = tag; hd.rs = rs; hd.tt = tt; hd.j = xj; hd.c4 = c4; hd.cgrp = cgrp;
    hd.kb = ws + WS_QKV + ((size_t)(32 + cgrp) * 2048) * 48; hd.vb = ws + WS_QKV + ((size_t)(64 + cgrp) * 2048) * 48;
    hd.qb = ws + WS_QKV + ((size_t)cgrp * 2048 + tt * 256) * 48;

    if (wave == 0) wait_flags(sy, SY_XF + tt * 64 + (fresh_lane() & 31), tag);
    wait_vm<0>();
    BBAR();
    STAMP(2);
    const int tile32 = tt * 8 + wave;
    if (c4 == 0) gemm1<0>(c, hd, ws, tile32);
    else if (c4 == 1) gemm1<1>(c, hd, ws, tile32);
    else if (c4 == 2) gemm1<2>(c, hd, ws, tile32);
    else gemm1<3>(c, hd, ws, tile32);
    STAMP(7);

    {
    const int lane = fresh_lane();
    const int hh = lane >> 5, blk = (lane >> 4) & 1, q = (lane & 15) >> 2, pp = lane & 3;
    const unsigned l16 = lane * 16;
    const bool tid0 = (wave == 0) && (lane == 0);
    const char* wdb = (const char*)wd + (size_t)(xj * 576) * 1152 + c4 * 288;
    const int mb = wave < 6 ? 4 * wave : 23;
    unsigned vo[4];
#pragma unroll
    for (int i = 0; i < 4; ++i) {
        const int n = (mb + i) * 64 + lane, row = n / 18, cc = n - row * 18;
        vo[i] = (unsigned)(row * 1152 + cc * 16 - 1024 * i);
    }
    auto issue_wd = [&](int slot, int dst) { glds16x4(wdb + (size_t)slot * 96 * 1152, vo, ldsbase + dst + mb * 1024); };
    const int q4 = lane >> 4, t16 = lane & 15, qq = (lane & 15) >> 2;
    const char* hsrc = ws + WS_H + ((size_t)tile32 * 288 + xj * 36) * 1024;
    const unsigned hvo = (unsigned)(((q4 >> 1) * 64 + (q4 & 1) * 32 + t16) * 16);
    bf16x8 hf[18][2];
    auto hload = [&](auto UI) { constexpr int u = UI, s = g2_ks(u);
        gload16_sc1<(s & 1) * 2048>(hf[u][0], hvo, hsrc + (s >> 1) * 4096);
        gload16_sc1<(s & 1) * 2048 + 256>(hf[u][1], hvo, hsrc + (s >> 1) * 4096); };
    unsigned hflag = 0;
    const int hfi = (xj * 8 + tt) * 64;
    {
        if (c4 == 0) wait_vm<1>(); else if (c4 == 1) wait_vm<2>(); else if (c4 == 2) wait_vm<3>(); else wait_vm<4>();
        BBAR();
        if (tid0) flag_store(sy, SY_HF + hfi + c4, tag);
        issue_wd(0, WQ0); issue_wd(1, WQ1); issue_wd(2, WQ2);
        STAMP(8);
        const int t = lane & 31;
        const int q0 = tt * 256 + wave * 32;
        bf16x8 Qf0, Qf1;
        {
            const unsigned qa = ldsbase + SPARE + (wave * 32 + t) * 48;
            Qf0 = __builtin_bit_cast(bf16x8, lds_rd128(qa + 16 * hh));
            const unsigned qv1 = lds_rd32(qa + 32);
            u32x4 q1 = {hh == 0 ? qv1 : 0u, 0u, 0u, 0u};
            Qf1 = __builtin_bit_cast(bf16x8, q1);
        }
        const unsigned kaddr = ldsbase + KPOS + (wave * 32 + t) * 48;
        const unsigned vaddr = ldsbase + VPOS + (wave * 32 + 4 * hh + q) * 48 + 32 * blk + 8 * pp;
        const float NEG = -__builtin_inff();
        f32x16 o;
        auto attend = [&](auto FULLC) {
            constexpr bool FULL = FULLC;
            const int ktmin = FULL ? 0 : 8 - wave;
            f32x16 sc[9];
#pragma unroll
            for (int kt = 0; kt < 9; ++kt) {
                if (FULL || kt >= ktmin) {
                    u32x4 k0 = lds_rd128(kaddr + kt * 1536 + 16 * hh);
                    u32x4 k1 = {lds_rd32(kaddr + kt * 1536 + 32), 0u, 0u, 0u};
                    f32x16 z;
#pragma unroll
                    for (int r = 0; r < 16; ++r) z[r] = 0.f;
                    z = MFMA32(__builtin_bit_cast(bf16x8, k0), Qf0, z);
                    sc[kt] = MFMA32(__builtin_bit_cast(bf16x8, k1), Qf1, z);
                } else {
#pragma unroll
                    for (int r = 0; r < 16; ++r) sc[kt][r] = NEG;
                }
            }
#pragma unroll
            for (int r = 0; r < 16; ++r) {
                const int kk = (r & 3) + 8 * (r >> 2) + 4 * hh;
                if (!(kk > t)) sc[0][r] = NEG;
                if (!(kk <= t)) sc[8][r] = NEG;
            }
            float m = NEG;
#pragma unroll
            for (int kt = 0; kt < 9; ++kt)
#pragma unroll
                for (int r = 0; r < 16; ++r) m = fmaxf(m, sc[kt][r]);
            m = xhalf_max(m);
            if (wave == 0) gload4_sc1(hflag, (unsigned)(SY_HF + hfi + (lane & 3)) * 4u, sy);
#pragma unroll
            for (int r = 0; r < 16; ++r) o[r] = 0.f;
            sfor<9>([&](auto KT) {
                constexpr int kt = KT;
                if constexpr (kt == 2) {
                    wait_vm<12>();
                    if (wave == 0) {
                        wait_vm_u<0>(hflag);
                        if (!__all(hflag == tag)) wait_flags(sy, SY_HF + hfi + (lane & 3), tag);
                    }
                    BBAR();
                    if (tid0) flag_store(sy, SY_H2 + hfi + c4, tag);
                }
                if constexpr (kt == 6) { if (wave == 0) gload4_sc1(hflag, (unsigned)(SY_H2 + hfi + (lane & 3)) * 4u, sy); }
                if (FULL || kt >= ktmin) {
                    unsigned pk[8];
#pragma unroll
                    for (int r = 0; r < 16; r += 2)
                        pk[r >> 1] = pk2(__builtin_amdgcn_exp2f(sc[kt][r] - m), __builtin_amdgcn_exp2f(sc[kt][r + 1] - m));
#pragma unroll
                    for (int s = 0; s < 2; ++s) {
                        u32x4 pb = {pk[4 * s], pk[4 * s + 1], pk[4 * s + 2], pk[4 * s + 3]};
                        bf16x8 A = tr_frag_o(vaddr, (kt * 32 + 16 * s) * 48, (kt * 32 + 16 * s + 8) * 48);
                        o = MFMA32(A, __builtin_bit_cast(bf16x8, pb), o);
                    }
                }
                if constexpr (kt >= 3) { SB(); hload(IC<2 * (kt - 3)>{}); hload(IC<2 * (kt - 3) + 1>{}); SB(); }
            });
        };
        if (tt > 0) attend(std::true_type{}); else attend(std::false_type{});
        const float inv = 1.f / lo_bcast(o[10]);
        const int tile16 = (q0 >> 4) + ((lane >> 4) & 1);
        const unsigned ob = (unsigned)WS_O + (unsigned)(tile16 * 18) * 1024u + t16 * 16;
        u32x4 w = {pk2(o[0] * inv, o[1] * inv), pk2(o[2] * inv, o[3] * inv), pk2(o[4] * inv, o[5] * inv), pk2(o[6] * inv, o[7] * inv)};
        st16_sc1(rs, ob + (cgrp >> 1) * 1024 + (2 * (cgrp & 1) + hh) * 256, w);
        st4_sc1(rs, hh == 0 ? ob + (16 + (cgrp >> 4)) * 1024 + ((cgrp >> 2) & 3) * 256 + (cgrp & 3) * 4 : 0xFFFFFFF0u, pk2(o[8] * inv, o[9] * inv));
        if (wave == 0) {
            wait_vm_u<14>(hflag);
            if (!__all(hflag == tag)) wait_flags(sy, SY_H2 + hfi + (lane & 3), tag);
        }
    }
    STAMP(9);
    f32x4 acc[2][9];
    const unsigned arel = ldsbase + (unsigned)((16 * (q4 >> 1) + 4 * (q4 & 1) + qq) * 288 + 8 * pp);
    const unsigned pdu = (unsigned)WS_P + (unsigned)(((tt * 4 + c4) * 8 + wave) * 8 + xj) * 9u * 1024u;
    const char* wob = (const char*)wo + c4 * 288;
    unsigned vw[4];
    auto wo_offsets = [&](bool last) {
        const int ln = fresh_lane();
#pragma unroll
        for (int i = 0; i < 4; ++i) {
            const int n = (mb + i) * 64 + ln, rs_ = n / 18, cc = n - rs_ * 18;
            const int u1 = rs_ >> 5, r1 = rs_ & 31, qx = 2 * (r1 >> 4) + ((r1 >> 2) & 1), tx = (r1 >> 3) & 1, qy = r1 & 3;
            const int f = 18 * (qx >> 1) + 4 * (qx & 1) + 8 * tx + qy;
            const int jx = 4 * tx + qy, g = 4 * (u1 - 1) + qx;
            const int row5 = (u1 == 0) ? 36 * 15 + f : 18 * (4 * g + (jx >> 1)) + 16 + (jx & 1);
            vw[i] = (unsigned)((last ? row5 : 36 * u1 + f) * 1152 + cc * 16 - 1024 * i);
        }
    };
    auto issue_wo = [&](int slot, int dst) { glds16x4(slot < 5 ? wob + (size_t)slot * 108 * 1152 : wob, vw, ldsbase + dst + mb * 1024); };
    {
        wait_vm<26>();
        BAR();
        constexpr int PF = 4;
        bf16x8 A[PF + 1];
        unsigned oflag = 0;
        constexpr int NS0 = 126, NST = 162;
        auto st_u = [](int idx) constexpr { return idx < NS0 ? idx / 9 : 14 + (idx - NS0) % 4; };
        auto st_dt = [](int idx) constexpr { return idx < NS0 ? idx % 9 : (idx - NS0) / 4; };
        unsigned cur = 0, curl = 0;
        auto rd = [&](auto IDX) {
            constexpr int idx = IDX, u = st_u(idx), dt = st_dt(idx);
            constexpr int ks = g2_ks(u), off = g2_place(ks / 3) + (ks % 3) * 32 * 288;
            if constexpr (idx < NS0) {
                if constexpr (dt == 0) cur = xor_add<0, off>(arel);
                return tr_frag_o(cur, dt * 32, dt * 32 + 8 * 288);
            } else {
                if constexpr (idx == NS0) { cur = xor_add<0, 0>(arel); curl = xor_add<0, WQ4>(arel); }
                if constexpr (off >= WQ4) return tr_frag_o(curl, off - WQ4 + dt * 32, off - WQ4 + dt * 32 + 8 * 288);
                else return tr_frag_o(cur, off + dt * 32, off + dt * 32 + 8 * 288);
            }
        };
        sfor<NST>([&](auto IDX) {
            constexpr int idx = IDX, u = st_u(idx), dt = st_dt(idx);
            if constexpr (idx < NS0 && dt == 0) {
                if constexpr (u == 0) STAMP(10);
                if constexpr (u == 3) { BBAR(); issue_wd(5, WQ0); }
                if constexpr (u == 6) {
                    wait_vm<12>();
                    sfor<6>([&](auto K) { pin(hf[6 + K][0]); pin(hf[6 + K][1]); });
                    BBAR();
                    if (tid0) flag_store(sy, SY_OF + tt * 64 + cgrp, tag);
                }
                if constexpr (u == 8) { if (wave == 0) gload4_sc1(oflag, (unsigned)(SY_OF + tt * 64 + (lane & 31)) * 4u, sy); }
                if constexpr (u == 10) {
                    wait_vm<8>();
                    if (wave == 0) {
                        wait_vm_u<0>(oflag);
                        if (!__all(oflag == tag)) wait_flags(sy, SY_OF + tt * 64 + (lane & 31), tag);
                        asm volatile("buffer_inv sc1" ::: "memory");
                    }
                    BBAR();
                }
                if constexpr (u == 12) {
                    if (wave == 0) wait_vm<1>(); else wait_vm<0>();
                    sfor<6>([&](auto K) { pin(hf[12 + K][0]); pin(hf[12 + K][1]); });
                    wo_offsets(false);
                }
                if constexpr (idx == 0) sfor<PF>([&](auto J) { A[J] = rd(J); });
                if constexpr (u < 6) wait_vm_dep2<ewait(u)>(hf[u][0], hf[u][1]);
            }
            if constexpr (idx == NS0) {
                if (wave == 0) wait_vm<0>();
                BBAR();
                const char* osrc = ws + WS_O + (size_t)((tt * 8 + xj) * 2) * 18 * 1024;
#pragma unroll
                for (int i = 0; i < 5; ++i) {
                    int m = wave + 8 * i; if (m >= 36) m -= 8;
                    glds16(osrc + m * 1024, l16, ldsbase + (m < 18 ? OTOP + m * 1024 : WQ3 + (m - 18) * 1024));
                }
                issue_wo(0, WQ1); issue_wo(1, WQ2);
            }
            if constexpr (idx + PF < NST) A[(idx + PF) % (PF + 1)] = rd(IC<idx + PF>{});
            SB();
            if constexpr (u == 0) {
                acc[0][dt] = MFMA16(A[idx % (PF + 1)], hf[u][0], ((f32x4){0.f, 0.f, 0.f, 0.f}));
                acc[1][dt] = MFMA16(A[idx % (PF + 1)], hf[u][1], ((f32x4){0.f, 0.f, 0.f, 0.f}));
            } else {
                acc[0][dt] = MFMA16(A[idx % (PF + 1)], hf[u][0], acc[0][dt]);
                acc[1][dt] = MFMA16(A[idx % (PF + 1)], hf[u][1], acc[1][dt]);
            }
            SB();
            if constexpr (idx < NS0 && dt == 2) {
                if constexpr (u == 0) issue_wd(3, WQ3);
                if constexpr (u == 1) { hload(IC<12>{}); hload(IC<13>{}); }
                if constexpr (u == 2) issue_wd(4, WQ4);
                if constexpr (u == 4) { hload(IC<14>{}); hload(IC<15>{}); }
                if constexpr (u == 5) { hload(IC<16>{}); hload(IC<17>{}); }
                SB();
            }
            if constexpr (idx >= NS0 && u == 16 && dt > 0) {
                constexpr int d = dt - 1;
                u32x4 w = {pk2(acc[0][d][0], acc[0][d][1]), pk2(acc[0][d][2], acc[0][d][3]), pk2(acc[1][d][0], acc[1][d][1]), pk2(acc[1][d][2], acc[1][d][3])};
                st16_sc1s(rs, l16, pdu + d * 1024, w);
                SB();
            }
        });
        STAMP(16);
        u32x4 w = {pk2(acc[0][8][0], acc[0][8][1]), pk2(acc[0][8][2], acc[0][8][3]), pk2(acc[1][8][0], acc[1][8][1]), pk2(acc[1][8][2], acc[1][8][3])};
        st16_sc1s(rs, l16, pdu + 8 * 1024, w);
    }
    {
        wait_vm<0>();
        BAR();
        if (tid0) flag_store(sy, SY_PF + (tt * 4 + c4) * 64 + xj, tag);
        STAMP(17);
        issue_wo(2, WQ0); issue_wo(3, WQ4);
        const int tok0 = tt * 256 + xj * 32;
        const unsigned psrc = (unsigned)WS_P + (unsigned)(((tt * 4 + c4) * 8 + xj) * 8) * 9u * 1024u + l16;
        auto fin = [&](auto NDC) {
            constexpr int ND = NDC;
            u32x2 xv[ND][2];
            u32x4 vy[ND][8];
            f32x4 sacc[ND][2];
            auto sgroup = [&](auto G) {
                constexpr int g = G;
                bf16x8 B0[3], B1[3], A[3][ND];
                auto rdk = [&](auto KK) {
                    constexpr int ks = 6 * g + KK, sl = ks / 3;
                    constexpr int place = sl == 0 ? WQ1 : sl == 1 ? WQ2 : sl == 2 ? WQ0 : sl == 3 ? WQ4 : sl == 4 ? WQ1 : WQ2;
                    B0[KK % 3] = __builtin_bit_cast(bf16x8, lds_rd128(ldsbase + OTOP + ks * 1024 + l16));
                    B1[KK % 3] = __builtin_bit_cast(bf16x8, lds_rd128(ldsbase + WQ3 + ks * 1024 + l16));
#pragma unroll
                    for (int k = 0; k < ND; ++k) A[KK % 3][k] = tr_frag_o(arel + place + (ks % 3) * 32 * 288 + ((k == 0) ? wave : 8) * 32, 0, 8 * 288);
                };
                rdk(IC<0>{}); rdk(IC<1>{});
                sfor<6>([&](auto KK) {
                    constexpr int kk = KK, ks = 6 * g + kk;
                    if constexpr (kk + 2 < 6) rdk(IC<kk + 2>{});
                    SB();
#pragma unroll
                    for (int k = 0; k < ND; ++k) {
                        if constexpr (ks == 0) { sacc[k][0] = MFMA16(A[kk % 3][k], B0[kk % 3], ((f32x4){0.f, 0.f, 0.f, 0.f})); sacc[k][1] = MFMA16(A[kk % 3][k], B1[kk % 3], ((f32x4){0.f, 0.f, 0.f, 0.f})); }
                        else { sacc[k][0] = MFMA16(A[kk % 3][k], B0[kk % 3], sacc[k][0]); sacc[k][1] = MFMA16(A[kk % 3][k], B1[kk % 3], sacc[k][1]); }
                    }
                    SB();
                });
            };
            sgroup(IC<0>{});
            BAR();
            glds16x4(wob + (size_t)4 * 108 * 1152, vw, ldsbase + WQ1 + mb * 1024);
            wo_offsets(true);
            issue_wo(5, WQ2);
#pragma unroll
            for (int k = 0; k < ND; ++k) {
                const int dt = (k == 0) ? wave : 8;
#pragma unroll
                for (int a = 0; a < 2; ++a)
                    xv[k][a] = *(const u32x2*)((const char*)x + (size_t)(tok0 + a * 16 + t16) * 1152 + (c4 * 144 + dt * 16 + 4 * q4) * 2);
            }
            wait_flags(sy, SY_PF + (tt * 4 + c4) * 64 + (lane & 7), tag);
            STAMP(18);
#pragma unroll
            for (int k = 0; k < ND; ++k) {
                const int dt = (k == 0) ? wave : 8;
#pragma unroll
                for (int sj = 0; sj < 8; ++sj) vy[k][sj] = __builtin_amdgcn_raw_buffer_load_b128(rs, psrc + (sj * 9 + dt) * 1024, 0, 16);
            }
            BBAR();
            sgroup(IC<1>{});
            sgroup(IC<2>{});
#pragma unroll
            for (int k = 0; k < ND; ++k) {
                const int dt = (k == 0) ? wave : 8;
                float y[8];
#pragma unroll
                for (int e = 0; e < 8; ++e) y[e] = 0.f;
#pragma unroll
                for (int sj = 0; sj < 8; ++sj)
#pragma unroll
                    for (int e = 0; e < 4; ++e) { y[2 * e] += lo2f(vy[k][sj][e]); y[2 * e + 1] += hi2f(vy[k][sj][e]); }
#pragma unroll
                for (int a = 0; a < 2; ++a) {
                    float ov[4];
#pragma unroll
                    for (int r = 0; r < 4; ++r) {
                        const float yy = rbf(y[4 * a + r]);
                        const float g = rbf(yy * __builtin_amdgcn_rcpf(1.f + __builtin_amdgcn_exp2f(-yy * 1.4426950408889634f)));
                        const float pr = rbf(rbf(sacc[k][a][r]) * g);
                        const unsigned xw = xv[k][a][r >> 1];
                        ov[r] = ((r & 1) ? hi2f(xw) : lo2f(xw)) + pr;
                    }
                    u32x2 w = {pk2(ov[0], ov[1]), pk2(ov[2], ov[3])};
                    *(u32x2*)((char*)out + (size_t)(tok0 + a * 16 + t16) * 1152 + (c4 * 144 + dt * 16 + 4 * q4) * 2) = w;
                }
            }
        };
        if (wave == 0) fin(IC<2>{}); else fin(IC<1>{});
        if (bid == 0 && wave == 7) {
            for (unsigned spins = 0; spins < kSpinCap; ++spins) {
                bool ok = true;
#pragma unroll
                for (int k = 0; k < 4; ++k) ok &= flag_load(sy, SY_START + lane + 64 * k) == tag;
                if (__all(ok)) break;
                __builtin_amdgcn_s_sleep(1);
            }
            if (lane == 0) flag_store(sy, SY_GEN, tag);
        }
    }
    }
    STAMP(19);
}

extern "C" void kernel_launch(void* const* d_in, const int* in_sizes, int n_in, void* d_out, int out_size, void* d_ws, size_t ws_size,
                              hipStream_t stream) {
    const u16* x = (const u16*)d_in[0];
    const u16* wqkv = (const u16*)d_in[1];
    const u16* wo = (const u16*)d_in[2];
    const u16* wg = (const u16*)d_in[4];
    const u16* wu = (const u16*)d_in[5];
    const u16* wd = (const u16*)d_in[6];
    (void)hipFuncSetAttribute((const void*)mega_kernel, hipFuncAttributeMaxDynamicSharedMemorySize, LDS_BYTES);
    mega_kernel<<<256, 512, LDS_BYTES, stream>>>(x, wqkv, wo, wg, wu, wd, (char*)d_ws, (u16*)d_out);
}
```

```cpp
#include <hip/hip_runtime.h>
#include <hip/hip_bf16.h>
#include <stdint.h>
#include <utility>

typedef unsigned short u16;
typedef __bf16 bf16x8 __attribute__((ext_vector_type(8)));
typedef __bf16 bf16x2 __attribute__((ext_vector_type(2)));
typedef float f32x16 __attribute__((ext_vector_type(16)));
typedef float f32x4 __attribute__((ext_vector_type(4)));
typedef float f32x2 __attribute__((ext_vector_type(2)));
typedef short s16x4 __attribute__((ext_vector_type(4)));
typedef short s16x8 __attribute__((ext_vector_type(8)));
typedef unsigned u32x4 __attribute__((ext_vector_type(4)));
typedef unsigned u32x2 __attribute__((ext_vector_type(2)));
#define LAS __attribute__((address_space(3)))
#define GAS __attribute__((address_space(1)))
#define DI __device__ __forceinline__

constexpr int S = 2048, D = 576, F = 4608;
constexpr size_t WS_SYNC = 0;
constexpr int SY_GEN = 0;
constexpr int SY_START = 64;
constexpr int SY_XF = 512;
constexpr int SY_QF = SY_XF + 8 * 64;
constexpr int SY_HF = SY_QF + 8 * 64;
constexpr int SY_OF = SY_HF + 64 * 64;
constexpr int SY_PF = SY_OF + 8 * 64;
constexpr int SY_H2 = SY_PF + 32 * 64;
constexpr int SY_END = SY_H2 + 64 * 64;
static_assert(SY_END * 4 <= 131072, "sync area");
constexpr size_t WS_H = 131072;
constexpr size_t WS_H_BYTES = (size_t)64 * 288 * 1024;
constexpr size_t WS_QKV = WS_H + WS_H_BYTES;
constexpr size_t WS_QKV_BYTES = (size_t)3 * 32 * 2048 * 48;
constexpr size_t WS_O = WS_QKV + WS_QKV_BYTES + 4096;
constexpr size_t WS_O_BYTES = (size_t)128 * 18 * 1024;
constexpr size_t WS_P = WS_O + WS_O_BYTES;
constexpr size_t WS_P_BYTES = (size_t)8 * 4 * 8 * 8 * 9 * 1024;
constexpr size_t WS_X = WS_P + WS_P_BYTES;
constexpr size_t WS_X_BYTES = (size_t)64 * 36 * 1024;
constexpr size_t WS_USED = WS_X + WS_X_BYTES;

DI float lo2f(unsigned v) { return __uint_as_float(v << 16); }
DI float hi2f(unsigned v) { return __uint_as_float(v & 0xffff0000u); }
DI unsigned pk2(float a, float b) { f32x2 t = {a, b}; return __builtin_bit_cast(unsigned, __builtin_convertvector(t, bf16x2)); }
DI float rbf(float f) { return lo2f(pk2(f, 0.f)); }
DI float dot2(unsigned a, unsigned b, float c) { return __builtin_amdgcn_fdot2_f32_bf16(__builtin_bit_cast(bf16x2, a), __builtin_bit_cast(bf16x2, b), c, false); }
DI float xhalf_max(float v) { auto r = __builtin_amdgcn_permlane32_swap(__float_as_uint(v), __float_as_uint(v), false, false); return fmaxf(__uint_as_float(r[0]), __uint_as_float(r[1])); }
DI float lo_bcast(float v) { auto r = __builtin_amdgcn_permlane32_swap(__float_as_uint(v), __float_as_uint(v), false, false); return __uint_as_float(r[0]); }

DI void glds16(const void* sbase, unsigned voff, unsigned lds_dst) {
    asm volatile("s_mov_b32 m0, %2\n\ts_nop 0\n\tglobal_load_lds_dwordx4 %0, %1" ::"v"(voff), "s"(sbase), "s"(lds_dst) : "memory");
}
DI void glds16x3(const void* sbase, unsigned v0, unsigned v1, unsigned v2, unsigned lds_dst) {
    asm volatile("s_mov_b32 m0, %4\n\ts_nop 0\n\tglobal_load_lds_dwordx4 %0, %3\n\tglobal_load_lds_dwordx4 %1, %3 offset:1024\n\tglobal_load_lds_dwordx4 %2, %3 offset:2048"
                 ::"v"(v0), "v"(v1), "v"(v2), "s"(sbase), "s"(lds_dst) : "memory");
}
DI void glds16x4(const void* sbase, const unsigned (&v)[4], unsigned lds_dst) {
    asm volatile("s_mov_b32 m0, %5\n\ts_nop 0\n\tglobal_load_lds_dwordx4 %0, %4\n\tglobal_load_lds_dwordx4 %1, %4 offset:1024\n\tglobal_load_lds_dwordx4 %2, %4 offset:2048\n\tglobal_load_lds_dwordx4 %3, %4 offset:3072"
                 ::"v"(v[0]), "v"(v[1]), "v"(v[2]), "v"(v[3]), "s"(sbase), "s"(lds_dst) : "memory");
}
template <int OFF> DI void gload16_sc1(bf16x8& dst, unsigned voff, const char* sbase) {
    asm volatile("global_load_dwordx4 %0, %1, %2 offset:%3 sc1" : "=v"(dst) : "v"(voff), "s"(sbase), "i"(OFF) : "memory");
}
DI void gload4_plain(unsigned& dst, unsigned voff, const void* sbase) {
    asm volatile("global_load_dword %0, %1, %2" : "+v"(dst) : "v"(voff), "s"(sbase) : "memory");
}
DI void gload4_sc1(unsigned& dst, unsigned voff, const void* sbase) {
    asm volatile("global_load_dword %0, %1, %2 sc1" : "=v"(dst) : "v"(voff), "s"(sbase) : "memory");
}
template <int N> DI void wait_vm_dep(bf16x8& v) { asm volatile("s_waitcnt vmcnt(%1)" : "+v"(v) : "i"(N)); }
template <int N> DI void wait_vm_dep2(bf16x8& v, bf16x8& w) { asm volatile("s_waitcnt vmcnt(%2)" : "+v"(v), "+v"(w) : "i"(N)); }
template <int N> DI void wait_vm_u(unsigned& v) { asm volatile("s_waitcnt vmcnt(%1)" : "+v"(v) : "i"(N)); }
template <int N> DI void wait_vm() { asm volatile("s_waitcnt vmcnt(%0)" ::"i"(N) : "memory"); }
#define WAIT_LGKM0() asm volatile("s_waitcnt lgkmcnt(0)" ::: "memory")
#define BAR() do { asm volatile("s_waitcnt lgkmcnt(0)" ::: "memory"); __builtin_amdgcn_s_barrier(); asm volatile("" ::: "memory"); } while (0)
#define BBAR() do { asm volatile("" ::: "memory"); __builtin_amdgcn_s_barrier(); asm volatile("" ::: "memory"); } while (0)
#define SB() __builtin_amdgcn_sched_barrier(0)
template <int... Is, class Fn> DI void sfor_impl(std::integer_sequence<int, Is...>, Fn&& f) { (f(std::integral_constant<int, Is>{}), ...); }
template <int N, class Fn> DI void sfor(Fn&& f) { sfor_impl(std::make_integer_sequence<int, N>{}, f); }
template <int V> using IC = std::integral_constant<int, V>;
DI void pin(bf16x8& v) { asm volatile("" : "+v"(v)); }
DI float glu_a(float g) { asm volatile("v_mul_f32 %0, 0xbfb8aa3b, %0\n\tv_exp_f32 %0, %0" : "+v"(g)); return g; }
DI float glu_b(float e, float u) { asm volatile("v_add_f32 %0, 1.0, %0\n\tv_rcp_f32 %0, %0\n\ts_nop 0\n\tv_mul_f32 %0, %0, %1" : "+v"(e) : "v"(u)); return e; }
template <int X, int ADD> DI unsigned xor_add(unsigned base) {
    unsigned r; asm volatile("v_xor_b32 %0, %2, %1\n\tv_add_u32 %0, %3, %0" : "=&v"(r) : "v"(base), "i"(X), "i"(ADD)); return r;
}
DI unsigned opaque(unsigned v) { asm volatile("" : "+v"(v)); return v; }
DI int fresh_lane() { int l; asm volatile("v_mbcnt_lo_u32_b32 %0, -1, 0\n\tv_mbcnt_hi_u32_b32 %0, -1, %0" : "=v"(l)); return l; }
DI bf16x8 tr_frag_o(unsigned base, int off0, int off1) {
    s16x4 lo = __builtin_amdgcn_ds_read_tr16_b64_v4i16((LAS s16x4*)((LAS char*)(size_t)base + off0));
    s16x4 hi = __builtin_amdgcn_ds_read_tr16_b64_v4i16((LAS s16x4*)((LAS char*)(size_t)base + off1));
    s16x8 f = __builtin_shufflevector(lo, hi, 0, 1, 2, 3, 4, 5, 6, 7);
    return __builtin_bit_cast(bf16x8, f);
}
DI u32x4 lds_rd128(unsigned a) { return *(LAS u32x4*)(size_t)a; }
DI unsigned lds_rd32(unsigned a) { return *(LAS unsigned*)(size_t)a; }
#define MFMA32(a, b, c) __builtin_amdgcn_mfma_f32_32x32x16_bf16((a), (b), (c), 0, 0, 0)
#define MFMA16(a, b, c) __builtin_amdgcn_mfma_f32_16x16x32_bf16((a), (b), (c), 0, 0, 0)

typedef __amdgpu_buffer_rsrc_t rsrc_t;
DI void st16_sc1(rsrc_t r, unsigned off, u32x4 v) { __builtin_amdgcn_raw_buffer_store_b128(v, r, off, 0, 16); }
DI void st16_sc1s(rsrc_t r, unsigned voff, unsigned soff, u32x4 v) {
    asm volatile("buffer_store_dwordx4 %0, %1, %2, %3 offen sc1\n\ts_nop 1" ::"v"(v), "v"(voff), "s"(r), "s"(soff) : "memory");
}
DI void st4_sc1(rsrc_t r, unsigned off, unsigned v) { __builtin_amdgcn_raw_buffer_store_b32(v, r, off, 0, 16); }
DI void flag_store(unsigned* sy, int idx, unsigned tag) { __hip_atomic_store((GAS unsigned*)sy + idx, tag, __ATOMIC_RELAXED, __HIP_MEMORY_SCOPE_AGENT); }
DI unsigned flag_load(const unsigned* sy, int idx) { return __hip_atomic_load((GAS unsigned*)sy + idx, __ATOMIC_RELAXED, __HIP_MEMORY_SCOPE_AGENT); }
constexpr unsigned kSpinCap = 1u << 20;
DI void wait_flags(const unsigned* sy, int idx, unsigned tag) {
    for (unsigned spins = 0; spins < kSpinCap; ++spins) {
        const unsigned v = flag_load(sy, idx);
        if (__all(v == tag)) break;
        __builtin_amdgcn_s_sleep(1);
    }
    asm volatile("" ::: "memory");
}

#define STAMP(i) do {} while (0)

template <int CTRL, int RMASK> DI float dpp_add(float v) {
    return v + __int_as_float(__builtin_amdgcn_update_dpp(0, __float_as_int(v), CTRL, RMASK, 0xf, true));
}
DI float wave_sum(float v) {
    v = dpp_add<0x111, 0xf>(v); v = dpp_add<0x112, 0xf>(v); v = dpp_add<0x114, 0xf>(v); v = dpp_add<0x118, 0xf>(v);
    v = dpp_add<0x142, 0xa>(v); v = dpp_add<0x143, 0xc>(v);
    return __int_as_float(__builtin_amdgcn_readlane(__float_as_int(v), 63));
}

constexpr int SLOT = 24576;
constexpr int SPARE = SLOT * 6;
constexpr int LDS_BYTES = 163840;
constexpr int KPOS = 4 * SLOT, VPOS = 5 * SLOT;
constexpr int WSL = 27648;
constexpr int WQ0 = 0, WQ1 = WSL, WQ2 = 2 * WSL, WQ3 = 3 * WSL, WQ4 = 4 * WSL, OTOP = 5 * WSL;
static_assert(WQ3 <= KPOS && OTOP + 18 * 1024 <= LDS_BYTES, "lds");

struct Ctx {
    const char* wq; const char* wgu;
    unsigned vq0, vq1, vq2, vg0, vg1, vg2;
    unsigned ldsbase;
    int wave; int P0;
    const char* wa; const char* wb; unsigned la, lb;
};
DI void issue_q(const Ctx& c, int chunk, int pos) { glds16x3(c.wq + (size_t)(chunk * 192) * 3456, c.vq0, c.vq1, c.vq2, c.ldsbase + pos * SLOT + c.wave * 3072); }
DI void issue_gu(const Ctx& c, int panel, int chunk, int pos) {
    glds16x3(c.wgu + (size_t)(chunk * 96) * 9216 + (size_t)(c.P0 + panel) * 128, c.vg0, c.vg1, c.vg2, c.ldsbase + pos * SLOT + c.wave * 3072);
}

constexpr int PF1 = 3;
DI void issue_slot(const Ctx& c, int m) {
    if (m < 6) issue_gu(c, 1, m, m % 6);
    else if (m < 9) issue_q(c, m - 6, m % 6);
    else if (m < 15) issue_gu(c, 0, m - 9, m % 6);
    else issue_gu(c, 2, m - 15, m % 6);
}

struct Hand {
    unsigned* sy; unsigned tag; rsrc_t rs; int tt, j, c4, cgrp;
    const char* kb; const char* vb; const char* qb;
};

constexpr int NSTEPS = 396;
struct Step { short slot, ks, t, set, tau, first, fmt; };
struct Sched {
    Step st[NSTEPS];
    short fill[NSTEPS + 100][3];
    short nrows;
    short qn;
    short qent[24][5];
    short qlast;
};
template <int C4> constexpr Sched make_sched() {
    Sched S{};
    const int NTa = 4 - C4, NTc = 1 + C4;
    int setsA[4] = {2, 3, 0, 1};
    int setsC[4] = {0, 0, 0, 0};
    {
        int k = 0;
        for (int s = 0; s < 4; ++s) { int order[4] = {3, 0, 1, 2}; int cand = order[s]; bool used = false; for (int t = 0; t < NTa; ++t) used |= (setsA[t] == cand); if (!used && k < NTc - 1) setsC[k++] = cand; }
        setsC[NTc - 1] = setsA[0];
    }
    int n = 0, lastStep[11] = {};
    for (int slot = 0; slot < 21; ++slot) {
        int pass = slot < 6 ? 0 : slot < 9 ? 1 : slot < 15 ? 2 : 3;
        int i = pass == 0 ? slot : pass == 1 ? slot - 6 : pass == 2 ? slot - 9 : slot - 15;
        int nt = pass == 0 ? 4 : pass == 1 ? 2 : pass == 2 ? NTa : NTc;
        int kps = pass == 1 ? 12 : 6;
        int nsl = pass == 1 ? 3 : 6;
        int tau0 = pass == 0 ? 0 : pass == 1 ? 4 : pass == 2 ? 6 : 6 + NTa;
        bool tmajor = (i == nsl - 1) || (i == 0 && pass > 0);
        for (int k = 0; k < nt * kps; ++k) {
            int t = tmajor ? k / kps : k % nt, s = tmajor ? k % kps : k / nt;
            Step e{};
            e.slot = slot; e.ks = i * kps + s; e.t = t; e.tau = tau0 + t; e.fmt = pass == 1; e.first = (e.ks == 0);
            e.set = pass == 0 ? t : pass == 1 ? t : pass == 2 ? setsA[t] : setsC[t];
            S.st[n] = e; lastStep[e.tau] = n; ++n;
        }
    }
    const int delta = C4 == 0 ? 0 : C4 == 1 ? 6 : C4 == 2 ? 4 : 2;
    int qn = 0;
    for (int sl = 0; sl < 3; ++sl)
        for (int t = 0; t < 2; ++t)
            for (int r = 0; r < 16; r += 2) {
                int m = 0, d0 = 0;
                for (int hh = 1; hh >= 0; --hh) { int nn = 32 * t + (r & 3) + 8 * (r >> 2) + 4 * hh - delta; if (nn >= 0 && nn < 54 && nn / 18 == sl) { m |= 1 << hh; d0 = nn % 18 - 4 * hh; } }
                if (m) { S.qent[qn][0] = t; S.qent[qn][1] = r; S.qent[qn][2] = sl; S.qent[qn][3] = m; S.qent[qn][4] = d0; ++qn; }
            }
    S.qn = qn;
    int freeg = 0;
    auto put = [&](int g, int code) { for (int k = 0; k < 3; ++k) if (S.fill[g][k] == 0) { S.fill[g][k] = code; return; } S.fill[g][0] = -1; };
    for (int tau = 0; tau < 11; ++tau) {
        if (tau == 4) continue;
        int ready = lastStep[tau] + 3;
        int g = ready > freeg ? ready : freeg;
        int dl = 100000;
        for (int k = lastStep[tau] + 1; k < NSTEPS; ++k) if (S.st[k].first && (S.st[k].set == S.st[lastStep[tau]].set || (tau == 5 && S.st[k].set == S.st[lastStep[4]].set))) { dl = k; break; }
        int ops[40] = {}, cnt = 0;
        if (tau == 5) {
            for (int sl = 0; sl < 3; ++sl) {
                for (int e = 0; e < qn; ++e) if (S.qent[e][2] == sl) ops[cnt++] = 2000 + e;
                ops[cnt++] = 2100 + sl; ops[cnt++] = 2110 + sl;
            }
        } else {
            const int b = 1000 + tau * 32;
            ops[cnt++] = b + 0;
            for (int j = 0; j < 7; ++j) { ops[cnt++] = b + j + 1; ops[cnt++] = b + 8 + j; }
            ops[cnt++] = b + 15; ops[cnt++] = b + 16; ops[cnt++] = b + 17;
        }
        int avail = dl - g, dbl = cnt > avail ? cnt - avail : 0;
        if (tau == 5 && dbl < cnt / 2) dbl = cnt / 2;
        int k = 0;
        while (k < cnt) { put(g, ops[k++]); if (dbl > 0 && k < cnt) { put(g, ops[k++]); --dbl; } ++g; }
        if (tau == 5) { for (int q = 0; q < NSTEPS + 100; ++q) for (int z = 0; z < 3; ++z) if (S.fill[q][z] == 2112) S.qlast = q; }
        freeg = g;
    }
    S.nrows = freeg;
    return S;
}
template <int C4> struct SchedOf { static constexpr Sched SC = make_sched<C4>(); };
template <int C4> constexpr int set_of(int tau) { for (int g = 0; g < NSTEPS; ++g) if (SchedOf<C4>::SC.st[g].tau == tau) return SchedOf<C4>::SC.st[g].set; return 0; }
constexpr int xpos(int s) { return s < 6 ? -1 : 4 * ((s - 6) / 3) + 2; }
constexpr int dpos(int d) {
    return d == 2 ? -1 : d == 3 ? 4 * 3 + 1 : d == 4 ? 4 * 9 + 1 : d == 5 ? 4 * 15 + 1 : d == 6 ? 4 * 21 + 1 : d == 7 ? 4 * 27 + 1 : d == 8 ? 24 * 5 + 1
         : d == 9 || d == 10 ? 24 * 6 + 1 : d == 11 || d == 12 ? 24 * 7 + 1 : 24 * (d - 5) + 1;
}
constexpr bool d_after(int e, int d) { return dpos(e) > dpos(d) || (dpos(e) == dpos(d) && e > d); }
constexpr int younger_than_x(int s, int now) {
    int n = 0;
    for (int t = s + 1; t < 36; ++t) if (xpos(t) < now) ++n;
    for (int d = 2; d <= 20; ++d) if (dpos(d) < now && (dpos(d) > xpos(s) || (dpos(d) == xpos(s) && d == 2))) n += 3;
    return n;
}
constexpr int younger_than_d(int d, int now) {
    int n = 0;
    for (int t = 0; t < 36; ++t) if (xpos(t) < now && xpos(t) > dpos(d)) ++n;
    for (int e = 2; e <= 20; ++e) if (dpos(e) < now && d_after(e, d)) n += 3;
    return n;
}

template <int C4>
DI void gemm1(const Ctx& c, const Hand& hd, char* ws, int tile32) {
#define S (SchedOf<C4>::SC)
    constexpr int NTa = 4 - C4;
    constexpr int NK = (C4 <= 1) ? 17 : (C4 == 2 ? 18 : 19), NV = (C4 <= 2) ? 18 : 19;
    constexpr int NCHK = C4 == 0 ? 14 : C4 == 1 ? 15 : C4 == 2 ? 17 : 18;
    constexpr int NQF = S.st[S.qlast < NSTEPS ? S.qlast : NSTEPS - 1].slot + 2;
    static_assert(NQF <= 13 && NQF >= 9, "qkv publish");
    unsigned Lgu, Lq = 0, l16;
    {
        const int lane = fresh_lane();
        const int hh = lane >> 5, blk = (lane >> 4) & 1, q = (lane & 15) >> 2, pp = lane & 3;
        Lgu = c.ldsbase + (unsigned)(blk * 12288 + (8 * hh + q) * 128 + 8 * pp + 32 * (((q >> 1) & 1) | (blk << 1)));
        l16 = lane * 16;
    }
    const unsigned hbs = (unsigned)WS_H + ((unsigned)tile32 * 288u + (unsigned)c.P0 * 4u) * 1024u;
    const unsigned stg = c.ldsbase + SPARE + c.wave * 2048;
    const unsigned qds = (unsigned)WS_QKV + (unsigned)((hd.cgrp * 3) * 2048 + tile32 * 32) * 48u;
    unsigned stw = 0;
    const char* xs = ws + WS_X + (size_t)tile32 * 36 * 1024;
    bf16x8 xf[36];
    f32x16 acc[4];
    unsigned cur[4];
    bf16x8 A[PF1 + 1];
    unsigned qflag = 0, pl0 = 0, pl1 = 0, warm0 = 0, warm1 = 0;
    sfor<6>([&](auto SI) { constexpr int s = SI; gload16_sc1<(s & 3) * 1024>(xf[s], l16, xs + (s >> 2) * 4096); });
    issue_slot(c, 2);
    auto rd = [&](auto IDX) {
        constexpr Step e = S.st[IDX];
        constexpr int kl = e.fmt ? e.ks % 12 : e.ks % 6, pos = e.slot % 6;
        constexpr int col = e.fmt ? e.t : (e.slot >= 9 && e.slot < 15 ? C4 + e.t : e.t);
        if constexpr (e.fmt && e.ks == 0 && e.t == 0) {
            const int lane = fresh_lane();
            const int hh = lane >> 5, blk = (lane >> 4) & 1, q = (lane & 15) >> 2, pp = lane & 3;
            Lq = c.ldsbase + (unsigned)((8 * hh + q) * 128 + 8 * pp + 32 * (blk ^ (((q >> 1) & 1) << 1)));
        }
        if constexpr (kl == 0) cur[e.t] = e.fmt ? xor_add<(col << 6), pos * SLOT>(Lq) : xor_add<(col << 5), pos * SLOT>(Lgu);
        return tr_frag_o(cur[e.t], kl * 2048, kl * 2048 + 512);
    };
    auto filler = [&](auto CODE) {
        constexpr int code = CODE;
        if constexpr (code >= 1000 && code < 2000) {
            constexpr int tau = (code - 1000) / 32, j = (code - 1000) % 32;
            constexpr int realset = set_of<C4>(tau);
            constexpr int fgo = tau < 4 ? 4 + tau : (tau < 6 + NTa ? C4 + tau - 6 : 8 + tau - 6 - NTa);
            if constexpr (j < 8) acc[realset][j] = glu_a(acc[realset][j]);
            else if constexpr (j < 16) acc[realset][j - 8] = glu_b(acc[realset][j - 8], acc[realset][j]);
            else if constexpr (j == 16) {
                pl0 = pk2(acc[realset][0], acc[realset][1]);
                pl1 = pk2(acc[realset][2], acc[realset][3]);
            } else {
                u32x4 w = {pl0, pl1, pk2(acc[realset][4], acc[realset][5]), pk2(acc[realset][6], acc[realset][7])};
                st16_sc1s(hd.rs, l16, hbs + fgo * 1024, w);
            }
        } else if constexpr (code >= 2000 && code < 2100) {
            constexpr int e = code - 2000, t = S.qent[e][0], r = S.qent[e][1], m = S.qent[e][3], d0 = S.qent[e][4];
            if constexpr (e == 0) { const int ln = fresh_lane(); stw = opaque(stg + (ln & 31) * 48 + ((ln >> 5) << 3)); }
            const float qs = (hd.cgrp * 3 + S.qent[e][2] < 32) ? 0.23570226039551584f * 1.4426950408889634f : 1.f;
            const unsigned v = pk2(acc[t][r] * qs, acc[t][r + 1] * qs);
            if constexpr (m == 3) *(LAS unsigned*)(size_t)(stw + 2 * d0) = v;
            else if ((int)((stw >> 3) & 1) == (m >> 1)) *(LAS unsigned*)(size_t)(stw + 2 * d0) = v;
        } else if constexpr (code >= 2100 && code < 2110) {
            constexpr int sl = code - 2100;
            if (((stw >> 3) & 1) == 0) *(LAS unsigned*)(size_t)(stw + 36) = (hd.cgrp * 3 + sl >= 64) ? 0x00003F80u : 0u;
            else *(LAS u32x2*)(size_t)(stw + 32) = (u32x2){0u, 0u};
        } else if constexpr (code >= 2110) {
            constexpr int sl = code - 2110;
            const u32x4 w0 = lds_rd128(stg + l16);
            st16_sc1s(hd.rs, l16, qds + (unsigned)sl * 2048u * 48u, w0);
            if (fresh_lane() < 32) { const u32x4 w1 = lds_rd128(stg + 1024 + l16); st16_sc1s(hd.rs, l16, qds + (unsigned)sl * 2048u * 48u + 1024, w1); }
        }
    };
    sfor<NSTEPS>([&](auto IDX) {
        constexpr int idx = IDX;
        constexpr Step e = S.st[idx];
        constexpr bool top = (idx == 0) || (S.st[idx > 0 ? idx - 1 : 0].slot != e.slot);
        if constexpr (top) {
            constexpr int n = e.slot;
            if constexpr (n == 5) {
                wait_vm<(younger_than_x(35, 120) < younger_than_d(6, 120) ? younger_than_x(35, 120) : younger_than_d(6, 120))>();
                pin(xf[30]); pin(xf[31]); pin(xf[32]); pin(xf[33]); pin(xf[34]); pin(xf[35]);
            } else if constexpr (n == NQF) wait_vm<3>();
            else if constexpr (n >= 1 && n <= 16) wait_vm<younger_than_d(n + 1, 24 * n)>();
            else if constexpr (n == 17) wait_vm<6>();
            else if constexpr (n == 18) wait_vm<3 + (NK <= 17 ? 3 : 0)>();
            else if constexpr (n == 19) wait_vm<(NK <= 18 ? 3 : 0) + (NV <= 18 ? 5 : 0)>();
            if constexpr (n == 7) {
                asm volatile("" ::"v"(warm0));
                const unsigned o = (unsigned)c.wave * 8192u + (unsigned)fresh_lane() * 128u;
                if (blockIdx.x < 92) gload4_plain(warm1, o < c.lb ? o : c.lb, c.wb);
            }
            if constexpr (n == 12) asm volatile("" ::"v"(warm1));
            if constexpr (n == NCHK) {
                if (c.wave == 0) {
                    wait_vm_u<(n + 4 <= 20 ? 3 : 0)>(qflag);
                    if (!__all(qflag == hd.tag)) { const int ln = fresh_lane(); wait_flags(hd.sy, SY_QF + ((ln >= 32 && hd.tt > 0) ? hd.tt - 1 : hd.tt) * 64 + (ln & 31), hd.tag); }
                    asm volatile("buffer_inv sc1" ::: "memory");
                }
            }
            if constexpr (n == NK) { if (c.wave == 0) wait_vm<(NCHK + 5 <= 20 ? 3 * (20 - (NCHK + 5) + 1) : 0)>(); }
            if constexpr (n > 0) BBAR();
            if constexpr (n == NQF) { if (c.wave == 0 && fresh_lane() == 0) flag_store(hd.sy, SY_QF + hd.tt * 64 + hd.cgrp, hd.tag); }
            if constexpr (n == NCHK - 1) { if (c.wave == 0) { const int lane = fresh_lane(); gload4_sc1(qflag, (unsigned)(SY_QF + ((lane >= 32 && hd.tt > 0) ? hd.tt - 1 : hd.tt) * 64 + (lane & 31)) * 4u, hd.sy); } }
            sfor<19>([&](auto DD) { constexpr int d = DD + 2; if constexpr (dpos(d) == 24 * n + 1 && n >= 5) issue_slot(c, d); });
            if constexpr (n == NK || n == NV) {
#pragma unroll
                for (int kv = 0; kv < 2; ++kv) {
                    if ((kv == 0 && n == NK) || (kv == 1 && n == NV)) {
#pragma unroll
                        for (int k = 0; k < 3; ++k) {
                            const int m = c.wave * 3 + k;
                            int off = (hd.tt * 256 - 256) * 48 + m * 1024;
                            if (off < 0) off = m * 1024;
                            glds16((kv == 0 ? hd.kb : hd.vb) + off, l16, c.ldsbase + (kv == 0 ? KPOS : VPOS) + m * 1024);
                        }
                    }
                }
            }
            if constexpr (n == NV) {
#pragma unroll
                for (int k = 0; k < 2; ++k) {
                    int m = c.wave * 2 + k; if (m > 11) m = 11;
                    glds16(hd.qb + m * 1024, l16, c.ldsbase + SPARE + m * 1024);
                }
            }
            if constexpr (idx == 0) sfor<PF1>([&](auto J) { A[J] = rd(J); });
        }
        if constexpr (idx + PF1 < NSTEPS) A[(idx + PF1) % (PF1 + 1)] = rd(IC<idx + PF1>{});
        if constexpr (e.slot < 5 && e.t == 0) {
            sfor<6>([&](auto DD) { constexpr int d = DD + 3; if constexpr (d <= 7 && dpos(d) == 4 * e.ks + 1) issue_slot(c, d); });
            sfor<3>([&](auto JJ) { constexpr int sx = 6 + 3 * e.ks + JJ; if constexpr (sx < 36) gload16_sc1<(sx & 3) * 1024>(xf[sx], l16, xs + (sx >> 2) * 4096); });
            if constexpr (e.ks == 16) {
                const unsigned o = (unsigned)c.wave * 8192u + (unsigned)fresh_lane() * 128u;
                if (blockIdx.x < 193) gload4_plain(warm0, o < c.la ? o : c.la, c.wa);
            }
            wait_vm_dep<younger_than_x(e.ks, 4 * e.ks + 3)>(xf[e.ks]);
        }
        SB();
        if constexpr (e.first) {
            f32x16 z;
#pragma unroll
            for (int r = 0; r < 16; ++r) z[r] = 0.f;
            acc[e.set] = MFMA32(A[idx % (PF1 + 1)], xf[e.ks], z);
        } else acc[e.set] = MFMA32(A[idx % (PF1 + 1)], xf[e.ks], acc[e.set]);
        SB();
        if constexpr (S.fill[idx][0] != 0) {
            filler(IC<S.fill[idx][0]>{});
            if constexpr (S.fill[idx][1] != 0) filler(IC<S.fill[idx][1]>{});
            if constexpr (S.fill[idx][2] != 0) filler(IC<S.fill[idx][2]>{});
            SB();
        }
    });
    sfor<S.nrows - NSTEPS>([&](auto R) {
        constexpr int g = NSTEPS + R;
        if constexpr (S.fill[g][0] != 0) filler(IC<S.fill[g][0]>{});
        if constexpr (S.fill[g][1] != 0) filler(IC<S.fill[g][1]>{});
        if constexpr (S.fill[g][2] != 0) filler(IC<S.fill[g][2]>{});
    });
}
#undef S

constexpr int g2_ks(int u) { constexpr int o[18] = {0, 1, 2, 3, 5, 6, 7, 9, 10, 11, 14, 15, 4, 8, 12, 13, 16, 17}; return o[u]; }
constexpr int g2_place(int sl) { return sl == 0 ? WQ0 : sl == 1 ? WQ1 : sl == 2 ? WQ2 : sl == 3 ? WQ3 : sl == 4 ? WQ4 : WQ0; }
constexpr int ewait(int u) { constexpr int c[8] = {0, 4, 8, 16, 16, 20, 24, 24}; return 2 * (11 - u) + 2 + c[u]; }

__global__ __launch_bounds__(512, 2) void mega_kernel(const u16* __restrict__ x, const u16* __restrict__ wqkv, const u16* __restrict__ wo,
                                                      const u16* __restrict__ wg, const u16* __restrict__ wu, const u16* __restrict__ wd,
                                                      char* __restrict__ ws, u16* __restrict__ out) {
    extern __shared__ __attribute__((aligned(16))) char smem[];
    const int tid = threadIdx.x;
    const int wave = __builtin_amdgcn_readfirstlane(tid >> 6);
    const int bid = blockIdx.x, xj = bid & 7, loc = bid >> 3;
    const int c4 = loc & 3, tt = loc >> 2, cgrp = xj * 4 + c4;
    const unsigned ldsbase = (unsigned)(size_t)(LAS char*)smem;
    unsigned* sy = (unsigned*)(ws + WS_SYNC);
    const rsrc_t rs = __builtin_amdgcn_make_buffer_rsrc(ws, 0, (unsigned)WS_USED, 0x00020000);
    unsigned tag;
    Ctx c;

    STAMP(0);
    {
        const int lane = tid & 63;
        const int xrow = bid * 8 + wave;
        const char* xr = (const char*)x + (size_t)xrow * 1152;
        u32x4 xv0 = *(const u32x4*)(xr + lane * 16);
        u32x4 xv1 = *(const u32x4*)(xr + 1024 + (lane & 7) * 16);
        const unsigned gen = flag_load(sy, SY_GEN);
        const int qa0 = (108 * cgrp) & ~15;
        c.wq = (const char*)wqkv + qa0; c.wgu = (const char*)((wave >> 2) ? wu : wg);
        c.ldsbase = ldsbase; c.wave = wave; c.P0 = (9 * cgrp) >> 2;
        {
            const int lrow = lane >> 3, c16 = lane & 7, bsel = wave >> 2;
            const int swgu = ((lrow >> 1) & 1) | (bsel << 1);
            const int chgu = (((c16 >> 1) ^ swgu) << 1) | (c16 & 1);
            c.vg0 = (unsigned)((24 * (wave & 3) + lrow) * 9216 + chgu * 16); c.vg1 = c.vg0 + 8 * 9216 - 1024; c.vg2 = c.vg0 + 16 * 9216 - 2048;
            const int swq = ((lrow >> 1) & 1) << 1;
            int chq = (((c16 >> 1) ^ swq) << 1) | (c16 & 1);
            if (cgrp == 31 && chq == 7) chq = 6;
            c.vq0 = (unsigned)((24 * wave + lrow) * 3456 + chq * 16); c.vq1 = c.vq0 + 8 * 3456 - 1024; c.vq2 = c.vq0 + 16 * 3456 - 2048;
        }
        tag = (unsigned)__builtin_amdgcn_readfirstlane((int)(gen * 0x9E3779B1u + 0x7F4A7C15u));
        if (bid < 81) { c.wa = (const char*)wg + (size_t)bid * 65536; c.la = 65536 - 128; }
        else if (bid < 162) { c.wa = (const char*)wu + (size_t)(bid - 81) * 65536; c.la = 65536 - 128; }
        else { const int k = (bid - 162) % 31; c.wa = (const char*)wqkv + (size_t)k * 65536; c.la = (k == 30) ? 1990656 - 30 * 65536 - 128 : 65536 - 128; }
        if (bid < 81) { c.wb = (const char*)wd + (size_t)bid * 65536; c.lb = 65536 - 128; }
        else { const int k = (bid - 81) % 11; c.wb = (const char*)wo + (size_t)k * 65536; c.lb = (k == 10) ? 663552 - 10 * 65536 - 128 : 65536 - 128; }
        float s1 = 0.f;
        if (lane >= 8) xv1 = (u32x4){0u, 0u, 0u, 0u};
#pragma unroll
        for (int k = 0; k < 4; ++k) { s1 = dot2(xv0[k], 0x3f803f80u, s1); s1 = dot2(xv1[k], 0x3f803f80u, s1); }
        const float meanf = wave_sum(s1) * (1.f / 576.f);
        float s2 = 0.f;
#pragma unroll
        for (int k = 0; k < 4; ++k) {
            float a = lo2f(xv0[k]) - meanf, b = hi2f(xv0[k]) - meanf; s2 += a * a + b * b;
            if (lane < 8) { a = lo2f(xv1[k]) - meanf; b = hi2f(xv1[k]) - meanf; s2 += a * a + b * b; }
        }
        const float var = rbf(wave_sum(s2) * (1.f / 576.f));
        const float mean = rbf(meanf);
        const float rden = 1.f / rbf(sqrtf(rbf(var + 1e-5f)));
        u32x4 o0, o1;
#pragma unroll
        for (int k = 0; k < 4; ++k) {
            o0[k] = pk2(rbf(lo2f(xv0[k]) - mean) * rden, rbf(hi2f(xv0[k]) - mean) * rden);
            o1[k] = pk2(rbf(lo2f(xv1[k]) - mean) * rden, rbf(hi2f(xv1[k]) - mean) * rden);
        }
        *(LAS u32x4*)(size_t)(ldsbase + SPARE + (lane * 8 + wave) * 16) = o0;
        if (lane < 8) *(LAS u32x4*)(size_t)(ldsbase + SPARE + ((64 + lane) * 8 + wave) * 16) = o1;
        BAR();
        const unsigned xd = (unsigned)WS_X + (unsigned)(bid >> 2) * 36u * 1024u;
        const int t0 = (bid & 3) * 8;
        {
            const int i = tid, cc = i >> 3, tk = i & 7;
            st16_sc1(rs, xd + ((cc >> 1) * 64 + (cc & 1) * 32 + t0 + tk) * 16, lds_rd128(ldsbase + SPARE + i * 16));
        }
        {
            const int i = 512 + lane, cc = i >> 3, tk = i & 7;
            st16_sc1(rs, wave == 0 ? xd + ((cc >> 1) * 64 + (cc & 1) * 32 + t0 + tk) * 16 : 0xFFFFFF00u, lds_rd128(ldsbase + SPARE + i * 16));
        }
        issue_slot(c, 0); issue_slot(c, 1);
        wait_vm<6>();
        BBAR();
        if (tid == 0) { flag_store(sy, SY_XF + (bid >> 5) * 64 + (bid & 31), tag); flag_store(sy, SY_START + bid, tag); }
    }
    STAMP(1);
    Hand hd;
    hd.sy = sy; hd.tag = tag; hd.rs = rs; hd.tt = tt; hd.j = xj; hd.c4 = c4; hd.cgrp = cgrp;
    hd.kb = ws + WS_QKV + ((size_t)(32 + cgrp) * 2048) * 48; hd.vb = ws + WS_QKV + ((size_t)(64 + cgrp) * 2048) * 48;
    hd.qb = ws + WS_QKV + ((size_t)cgrp * 2048 + tt * 256) * 48;

    if (wave == 0) wait_flags(sy, SY_XF + tt * 64 + (fresh_lane() & 31), tag);
    wait_vm<0>();
    BBAR();
    STAMP(2);
    const int tile32 = tt * 8 + wave;
    if (c4 == 0) gemm1<0>(c, hd, ws, tile32);
    else if (c4 == 1) gemm1<1>(c, hd, ws, tile32);
    else if (c4 == 2) gemm1<2>(c, hd, ws, tile32);
    else gemm1<3>(c, hd, ws, tile32);
    STAMP(7);

    {
    const int lane = fresh_lane();
    const int hh = lane >> 5, blk = (lane >> 4) & 1, q = (lane & 15) >> 2, pp = lane & 3;
    const unsigned l16 = lane * 16;
    const bool tid0 = (wave == 0) && (lane == 0);
    const char* wdb = (const char*)wd + (size_t)(xj * 576) * 1152 + c4 * 288;
    const int mb = wave < 6 ? 4 * wave : 23;
    unsigned vo[4];
#pragma unroll
    for (int i = 0; i < 4; ++i) {
        const int n = (mb + i) * 64 + lane, row = n / 18, cc = n - row * 18;
        vo[i] = (unsigned)(row * 1152 + cc * 16 - 1024 * i);
    }
    auto issue_wd = [&](int slot, int dst) { glds16x4(wdb + (size_t)slot * 96 * 1152, vo, ldsbase + dst + mb * 1024); };
    const int q4 = lane >> 4, t16 = lane & 15, qq = (lane & 15) >> 2;
    const char* hsrc = ws + WS_H + ((size_t)tile32 * 288 + xj * 36) * 1024;
    const unsigned hvo = (unsigned)(((q4 >> 1) * 64 + (q4 & 1) * 32 + t16) * 16);
    bf16x8 hf[18][2];
    auto hload = [&](auto UI) { constexpr int u = UI, s = g2_ks(u);
        gload16_sc1<(s & 1) * 2048>(hf[u][0], hvo, hsrc + (s >> 1) * 4096);
        gload16_sc1<(s & 1) * 2048 + 256>(hf[u][1], hvo, hsrc + (s >> 1) * 4096); };
    unsigned hflag = 0;
    const int hfi = (xj * 8 + tt) * 64;
    {
        if (c4 == 0) wait_vm<1>(); else if (c4 == 1) wait_vm<2>(); else if (c4 == 2) wait_vm<3>(); else wait_vm<4>();
        BBAR();
        if (tid0) flag_store(sy, SY_HF + hfi + c4, tag);
        issue_wd(0, WQ0); issue_wd(1, WQ1); issue_wd(2, WQ2);
        STAMP(8);
        const int t = lane & 31;
        const int q0 = tt * 256 + wave * 32;
        bf16x8 Qf0, Qf1;
        {
            const unsigned qa = ldsbase + SPARE + (wave * 32 + t) * 48;
            Qf0 = __builtin_bit_cast(bf16x8, lds_rd128(qa + 16 * hh));
            const unsigned qv1 = lds_rd32(qa + 32);
            u32x4 q1 = {hh == 0 ? qv1 : 0u, 0u, 0u, 0u};
            Qf1 = __builtin_bit_cast(bf16x8, q1);
        }
        const unsigned kaddr = ldsbase + KPOS + (wave * 32 + t) * 48;
        const unsigned vaddr = ldsbase + VPOS + (wave * 32 + 4 * hh + q) * 48 + 32 * blk + 8 * pp;
        const float NEG = -__builtin_inff();
        f32x16 o;
        auto attend = [&](auto FULLC) {
            constexpr bool FULL = FULLC;
            const int ktmin = FULL ? 0 : 8 - wave;
            f32x16 sc[9];
#pragma unroll
            for (int kt = 0; kt < 9; ++kt) {
                if (FULL || kt >= ktmin) {
                    u32x4 k0 = lds_rd128(kaddr + kt * 1536 + 16 * hh);
                    u32x4 k1 = {lds_rd32(kaddr + kt * 1536 + 32), 0u, 0u, 0u};
                    f32x16 z;
#pragma unroll
                    for (int r = 0; r < 16; ++r) z[r] = 0.f;
                    z = MFMA32(__builtin_bit_cast(bf16x8, k0), Qf0, z);
                    sc[kt] = MFMA32(__builtin_bit_cast(bf16x8, k1), Qf1, z);
                } else {
#pragma unroll
                    for (int r = 0; r < 16; ++r) sc[kt][r] = NEG;
                }
            }
#pragma unroll
            for (int r = 0; r < 16; ++r) {
                const int kk = (r & 3) + 8 * (r >> 2) + 4 * hh;
                if (!(kk > t)) sc[0][r] = NEG;
                if (!(kk <= t)) sc[8][r] = NEG;
            }
            float m = NEG;
#pragma unroll
            for (int kt = 0; kt < 9; ++kt)
#pragma unroll
                for (int r = 0; r < 16; ++r) m = fmaxf(m, sc[kt][r]);
            m = xhalf_max(m);
            if (wave == 0) gload4_sc1(hflag, (unsigned)(SY_HF + hfi + (lane & 3)) * 4u, sy);
#pragma unroll
            for (int r = 0; r < 16; ++r) o[r] = 0.f;
            sfor<9>([&](auto KT) {
                constexpr int kt = KT;
                if constexpr (kt == 2) {
                    wait_vm<12>();
                    if (wave == 0) {
                        wait_vm_u<0>(hflag);
                        if (!__all(hflag == tag)) wait_flags(sy, SY_HF + hfi + (lane & 3), tag);
                    }
                    BBAR();
                    if (tid0) flag_store(sy, SY_H2 + hfi + c4, tag);
                }
                if constexpr (kt == 6) { if (wave == 0) gload4_sc1(hflag, (unsigned)(SY_H2 + hfi + (lane & 3)) * 4u, sy); }
                if (FULL || kt >= ktmin) {
                    unsigned pk[8];
#pragma unroll
                    for (int r = 0; r < 16; r += 2)
                        pk[r >> 1] = pk2(__builtin_amdgcn_exp2f(sc[kt][r] - m), __builtin_amdgcn_exp2f(sc[kt][r + 1] - m));
#pragma unroll
                    for (int s = 0; s < 2; ++s) {
                        u32x4 pb = {pk[4 * s], pk[4 * s + 1], pk[4 * s + 2], pk[4 * s + 3]};
                        bf16x8 A = tr_frag_o(vaddr, (kt * 32 + 16 * s) * 48, (kt * 32 + 16 * s + 8) * 48);
                        o = MFMA32(A, __builtin_bit_cast(bf16x8, pb), o);
                    }
                }
                if constexpr (kt >= 3) { SB(); hload(IC<2 * (kt - 3)>{}); hload(IC<2 * (kt - 3) + 1>{}); SB(); }
            });
        };
        if (tt > 0) attend(std::true_type{}); else attend(std::false_type{});
        const float inv = 1.f / lo_bcast(o[10]);
        const int tile16 = (q0 >> 4) + ((lane >> 4) & 1);
        const unsigned ob = (unsigned)WS_O + (unsigned)(tile16 * 18) * 1024u + t16 * 16;
        u32x4 w = {pk2(o[0] * inv, o[1] * inv), pk2(o[2] * inv, o[3] * inv), pk2(o[4] * inv, o[5] * inv), pk2(o[6] * inv, o[7] * inv)};
        st16_sc1(rs, ob + (cgrp >> 1) * 1024 + (2 * (cgrp & 1) + hh) * 256, w);
        st4_sc1(rs, hh == 0 ? ob + (16 + (cgrp >> 4)) * 1024 + ((cgrp >> 2) & 3) * 256 + (cgrp & 3) * 4 : 0xFFFFFFF0u, pk2(o[8] * inv, o[9] * inv));
        if (wave == 0) {
            wait_vm_u<14>(hflag);
            if (!__all(hflag == tag)) wait_flags(sy, SY_H2 + hfi + (lane & 3), tag);
        }
    }
    STAMP(9);
    f32x4 acc[2][9];
    const unsigned arel = ldsbase + (unsigned)((16 * (q4 >> 1) + 4 * (q4 & 1) + qq) * 288 + 8 * pp);
    const unsigned pdu = (unsigned)WS_P + (unsigned)(((tt * 4 + c4) * 8 + wave) * 8 + xj) * 9u * 1024u;
    const char* wob = (const char*)wo + c4 * 288;
    unsigned vw[4];
    auto wo_offsets = [&](bool last) {
        const int ln = fresh_lane();
#pragma unroll
        for (int i = 0; i < 4; ++i) {
            const int n = (mb + i) * 64 + ln, rs_ = n / 18, cc = n - rs_ * 18;
            const int u1 = rs_ >> 5, r1 = rs_ & 31, qx = 2 * (r1 >> 4) + ((r1 >> 2) & 1), tx = (r1 >> 3) & 1, qy = r1 & 3;
            const int f = 18 * (qx >> 1) + 4 * (qx & 1) + 8 * tx + qy;
            const int jx = 4 * tx + qy, g = 4 * (u1 - 1) + qx;
            const int row5 = (u1 == 0) ? 36 * 15 + f : 18 * (4 * g + (jx >> 1)) + 16 + (jx & 1);
            vw[i] = (unsigned)((last ? row5 : 36 * u1 + f) * 1152 + cc * 16 - 1024 * i);
        }
    };
    auto issue_wo = [&](int slot, int dst) { glds16x4(slot < 5 ? wob + (size_t)slot * 108 * 1152 : wob, vw, ldsbase + dst + mb * 1024); };
    {
        wait_vm<26>();
        BAR();
        constexpr int PF = 4;
        bf16x8 A[PF + 1];
        unsigned oflag = 0;
        constexpr int NS0 = 126, NST = 162;
        auto st_u = [](int idx) constexpr { return idx < NS0 ? idx / 9 : 14 + (idx - NS0) % 4; };
        auto st_dt = [](int idx) constexpr { return idx < NS0 ? idx % 9 : (idx - NS0) / 4; };
        unsigned cur = 0, curl = 0;
        auto rd = [&](auto IDX) {
            constexpr int idx = IDX, u = st_u(idx), dt = st_dt(idx);
            constexpr int ks = g2_ks(u), off = g2_place(ks / 3) + (ks % 3) * 32 * 288;
            if constexpr (idx < NS0) {
                if constexpr (dt == 0) cur = xor_add<0, off>(arel);
                return tr_frag_o(cur, dt * 32, dt * 32 + 8 * 288);
            } else {
                if constexpr (idx == NS0) { cur = xor_add<0, 0>(arel); curl = xor_add<0, WQ4>(arel); }
                if constexpr (off >= WQ4) return tr_frag_o(curl, off - WQ4 + dt * 32, off - WQ4 + dt * 32 + 8 * 288);
                else return tr_frag_o(cur, off + dt * 32, off + dt * 32 + 8 * 288);
            }
        };
        sfor<NST>([&](auto IDX) {
            constexpr int idx = IDX, u = st_u(idx), dt = st_dt(idx);
            if constexpr (idx < NS0 && dt == 0) {
                if constexpr (u == 0) STAMP(10);
                if constexpr (u == 3) { BBAR(); issue_wd(5, WQ0); }
                if constexpr (u == 6) {
                    wait_vm<12>();
                    sfor<6>([&](auto K) { pin(hf[6 + K][0]); pin(hf[6 + K][1]); });
                    BBAR();
                    if (tid0) flag_store(sy, SY_OF + tt * 64 + cgrp, tag);
                }
                if constexpr (u == 8) { if (wave == 0) gload4_sc1(oflag, (unsigned)(SY_OF + tt * 64 + (lane & 31)) * 4u, sy); }
                if constexpr (u == 10) {
                    wait_vm<8>();
                    if (wave == 0) {
                        wait_vm_u<0>(oflag);
                        if (!__all(oflag == tag)) wait_flags(sy, SY_OF + tt * 64 + (lane & 31), tag);
                        asm volatile("buffer_inv sc1" ::: "memory");
                    }
                    BBAR();
                }
                if constexpr (u == 12) {
                    if (wave == 0) wait_vm<1>(); else wait_vm<0>();
                    sfor<6>([&](auto K) { pin(hf[12 + K][0]); pin(hf[12 + K][1]); });
                    wo_offsets(false);
                }
                if constexpr (idx == 0) sfor<PF>([&](auto J) { A[J] = rd(J); });
                if constexpr (u < 6) wait_vm_dep2<ewait(u)>(hf[u][0], hf[u][1]);
            }
            if constexpr (idx == NS0) {
                if (wave == 0) wait_vm<0>();
                BBAR();
                const char* osrc = ws + WS_O + (size_t)((tt * 8 + xj) * 2) * 18 * 1024;
#pragma unroll
                for (int i = 0; i < 5; ++i) {
                    int m = wave + 8 * i; if (m >= 36) m -= 8;
                    glds16(osrc + m * 1024, l16, ldsbase + (m < 18 ? OTOP + m * 1024 : WQ3 + (m - 18) * 1024));
                }
                issue_wo(0, WQ1); issue_wo(1, WQ2);
            }
            if constexpr (idx + PF < NST) A[(idx + PF) % (PF + 1)] = rd(IC<idx + PF>{});
            SB();
            if constexpr (u == 0) {
                acc[0][dt] = MFMA16(A[idx % (PF + 1)], hf[u][0], ((f32x4){0.f, 0.f, 0.f, 0.f}));
                acc[1][dt] = MFMA16(A[idx % (PF + 1)], hf[u][1], ((f32x4){0.f, 0.f, 0.f, 0.f}));
            } else {
                acc[0][dt] = MFMA16(A[idx % (PF + 1)], hf[u][0], acc[0][dt]);
                acc[1][dt] = MFMA16(A[idx % (PF + 1)], hf[u][1], acc[1][dt]);
            }
            SB();
            if constexpr (idx < NS0 && dt == 2) {
                if constexpr (u == 0) issue_wd(3, WQ3);
                if constexpr (u == 1) { hload(IC<12>{}); hload(IC<13>{}); }
                if constexpr (u == 2) issue_wd(4, WQ4);
                if constexpr (u == 4) { hload(IC<14>{}); hload(IC<15>{}); }
                if constexpr (u == 5) { hload(IC<16>{}); hload(IC<17>{}); }
                SB();
            }
            if constexpr (idx >= NS0 && u == 16 && dt > 0) {
                constexpr int d = dt - 1;
                u32x4 w = {pk2(acc[0][d][0], acc[0][d][1]), pk2(acc[0][d][2], acc[0][d][3]), pk2(acc[1][d][0], acc[1][d][1]), pk2(acc[1][d][2], acc[1][d][3])};
                st16_sc1s(rs, l16, pdu + d * 1024, w);
                SB();
            }
        });
        STAMP(16);
        u32x4 w = {pk2(acc[0][8][0], acc[0][8][1]), pk2(acc[0][8][2], acc[0][8][3]), pk2(acc[1][8][0], acc[1][8][1]), pk2(acc[1][8][2], acc[1][8][3])};
        st16_sc1s(rs, l16, pdu + 8 * 1024, w);
    }
    {
        wait_vm<0>();
        BAR();
        if (tid0) flag_store(sy, SY_PF + (tt * 4 + c4) * 64 + xj, tag);
        STAMP(17);
        issue_wo(2, WQ0); issue_wo(3, WQ4);
        const int tok0 = tt * 256 + xj * 32;
        const unsigned psrc = (unsigned)WS_P + (unsigned)(((tt * 4 + c4) * 8 + xj) * 8) * 9u * 1024u + l16;
        auto fin = [&](auto NDC) {
            constexpr int ND = NDC;
            u32x2 xv[ND][2];
            u32x4 vy[ND][8];
            f32x4 sacc[ND][2];
            auto sgroup = [&](auto G) {
                constexpr int g = G;
                bf16x8 B0[3], B1[3], A[3][ND];
                auto rdk = [&](auto KK) {
                    constexpr int ks = 6 * g + KK, sl = ks / 3;
                    constexpr int place = sl == 0 ? WQ1 : sl == 1 ? WQ2 : sl == 2 ? WQ0 : sl == 3 ? WQ4 : sl == 4 ? WQ1 : WQ2;
                    B0[KK % 3] = __builtin_bit_cast(bf16x8, lds_rd128(ldsbase + OTOP + ks * 1024 + l16));
                    B1[KK % 3] = __builtin_bit_cast(bf16x8, lds_rd128(ldsbase + WQ3 + ks * 1024 + l16));
#pragma unroll
                    for (int k = 0; k < ND; ++k) A[KK % 3][k] = tr_frag_o(arel + place + (ks % 3) * 32 * 288 + ((k == 0) ? wave : 8) * 32, 0, 8 * 288);
                };
                rdk(IC<0>{}); rdk(IC<1>{});
                sfor<6>([&](auto KK) {
                    constexpr int kk = KK, ks = 6 * g + kk;
                    if constexpr (kk + 2 < 6) rdk(IC<kk + 2>{});
                    SB();
#pragma unroll
                    for (int k = 0; k < ND; ++k) {
                        if constexpr (ks == 0) { sacc[k][0] = MFMA16(A[kk % 3][k], B0[kk % 3], ((f32x4){0.f, 0.f, 0.f, 0.f})); sacc[k][1] = MFMA16(A[kk % 3][k], B1[kk % 3], ((f32x4){0.f, 0.f, 0.f, 0.f})); }
                        else { sacc[k][0] = MFMA16(A[kk % 3][k], B0[kk % 3], sacc[k][0]); sacc[k][1] = MFMA16(A[kk % 3][k], B1[kk % 3], sacc[k][1]); }
                    }
                    SB();
                });
            };
            sgroup(IC<0>{});
            BAR();
            glds16x4(wob + (size_t)4 * 108 * 1152, vw, ldsbase + WQ1 + mb * 1024);
            wo_offsets(true);
            issue_wo(5, WQ2);
#pragma unroll
            for (int k = 0; k < ND; ++k) {
                const int dt = (k == 0) ? wave : 8;
#pragma unroll
                for (int a = 0; a < 2; ++a)
                    xv[k][a] = *(const u32x2*)((const char*)x + (size_t)(tok0 + a * 16 + t16) * 1152 + (c4 * 144 + dt * 16 + 4 * q4) * 2);
            }
            wait_flags(sy, SY_PF + (tt * 4 + c4) * 64 + (lane & 7), tag);
            STAMP(18);
#pragma unroll
            for (int k = 0; k < ND; ++k) {
                const int dt = (k == 0) ? wave : 8;
#pragma unroll
                for (int sj = 0; sj < 8; ++sj) vy[k][sj] = __builtin_amdgcn_raw_buffer_load_b128(rs, psrc + (sj * 9 + dt) * 1024, 0, 16);
            }
            BBAR();
            sgroup(IC<1>{});
            sgroup(IC<2>{});
#pragma unroll
            for (int k = 0; k < ND; ++k) {
                const int dt = (k == 0) ? wave : 8;
                float y[8];
#pragma unroll
                for (int e = 0; e < 8; ++e) y[e] = 0.f;
#pragma unroll
                for (int sj = 0; sj < 8; ++sj)
#pragma unroll
                    for (int e = 0; e < 4; ++e) { y[2 * e] += lo2f(vy[k][sj][e]); y[2 * e + 1] += hi2f(vy[k][sj][e]); }
#pragma unroll
                for (int a = 0; a < 2; ++a) {
                    float ov[4];
#pragma unroll
                    for (int r = 0; r < 4; ++r) {
                        const float yy = rbf(y[4 * a + r]);
                        const float g = rbf(yy * __builtin_amdgcn_rcpf(1.f + __builtin_amdgcn_exp2f(-yy * 1.4426950408889634f)));
                        const float pr = rbf(rbf(sacc[k][a][r]) * g);
                        const unsigned xw = xv[k][a][r >> 1];
                        ov[r] = ((r & 1) ? hi2f(xw) : lo2f(xw)) + pr;
                    }
                    u32x2 w = {pk2(ov[0], ov[1]), pk2(ov[2], ov[3])};
                    *(u32x2*)((char*)out + (size_t)(tok0 + a * 16 + t16) * 1152 + (c4 * 144 + dt * 16 + 4 * q4) * 2) = w;
                }
            }
        };
        if (wave == 0) fin(IC<2>{}); else fin(IC<1>{});
        if (bid == 0 && wave == 7) {
            for (unsigned spins = 0; spins < kSpinCap; ++spins) {
                bool ok = true;
#pragma unroll
                for (int k = 0; k < 4; ++k) ok &= flag_load(sy, SY_START + lane + 64 * k) == tag;
                if (__all(ok)) break;
                __builtin_amdgcn_s_sleep(1);
            }
            if (lane == 0) flag_store(sy, SY_GEN, tag);
        }
    }
    }
    STAMP(19);
}

extern "C" void kernel_launch(void* const* d_in, const int* in_sizes, int n_in, void* d_out, int out_size, void* d_ws, size_t ws_size,
                              hipStream_t stream) {
    const u16* x = (const u16*)d_in[0];
    const u16* wqkv = (const u16*)d_in[1];
    const u16* wo = (const u16*)d_in[2];
    const u16* wg = (const u16*)d_in[4];
    const u16* wu = (const u16*)d_in[5];
    const u16* wd = (const u16*)d_in[6];
    (void)hipFuncSetAttribute((const void*)mega_kernel, hipFuncAttributeMaxDynamicSharedMemorySize, LDS_BYTES);
    mega_kernel<<<256, 512, LDS_BYTES, stream>>>(x, wqkv, wo, wg, wu, wd, (char*)d_ws, (u16*)d_out);
}
```

```cpp
#include <hip/hip_runtime.h>
#include <hip/hip_bf16.h>
#include <stdint.h>
#include <utility>

typedef unsigned short u16;
typedef __bf16 bf16x8 __attribute__((ext_vector_type(8)));
typedef __bf16 bf16x2 __attribute__((ext_vector_type(2)));
typedef float f32x16 __attribute__((ext_vector_type(16)));
typedef float f32x4 __attribute__((ext_vector_type(4)));
typedef float f32x2 __attribute__((ext_vector_type(2)));
typedef short s16x4 __attribute__((ext_vector_type(4)));
typedef short s16x8 __attribute__((ext_vector_type(8)));
typedef unsigned u32x4 __attribute__((ext_vector_type(4)));
typedef unsigned u32x2 __attribute__((ext_vector_type(2)));
#define LAS __attribute__((address_space(3)))
#define GAS __attribute__((address_space(1)))
#define DI __device__ __forceinline__

constexpr int S = 2048, D = 576, F = 4608;
constexpr size_t WS_SYNC = 0;
constexpr int SY_GEN = 0;
constexpr int SY_START = 64;
constexpr int SY_XF = 512;
constexpr int SY_QF = SY_XF + 8 * 64;
constexpr int SY_HF = SY_QF + 8 * 64;
constexpr int SY_OF = SY_HF + 64 * 64;
constexpr int SY_PF = SY_OF + 8 * 64;
constexpr int SY_H2 = SY_PF + 32 * 64;
constexpr int SY_END = SY_H2 + 64 * 64;
static_assert(SY_END * 4 <= 131072, "sync area");
constexpr size_t WS_H = 131072;
constexpr size_t WS_H_BYTES = (size_t)64 * 288 * 1024;
constexpr size_t WS_QKV = WS_H + WS_H_BYTES;
constexpr size_t WS_QKV_BYTES = (size_t)3 * 32 * 2048 * 48;
constexpr size_t WS_O = WS_QKV + WS_QKV_BYTES + 4096;
constexpr size_t WS_O_BYTES = (size_t)128 * 18 * 1024;
constexpr size_t WS_P = WS_O + WS_O_BYTES;
constexpr size_t WS_P_BYTES = (size_t)8 * 4 * 8 * 8 * 9 * 1024;
constexpr size_t WS_X = WS_P + WS_P_BYTES;
constexpr size_t WS_X_BYTES = (size_t)64 * 36 * 1024;
constexpr size_t WS_USED = WS_X + WS_X_BYTES;

DI float lo2f(unsigned v) { return __uint_as_float(v << 16); }
DI float hi2f(unsigned v) { return __uint_as_float(v & 0xffff0000u); }
DI unsigned pk2(float a, float b) { f32x2 t = {a, b}; return __builtin_bit_cast(unsigned, __builtin_convertvector(t, bf16x2)); }
DI float rbf(float f) { return lo2f(pk2(f, 0.f)); }
DI float dot2(unsigned a, unsigned b, float c) { return __builtin_amdgcn_fdot2_f32_bf16(__builtin_bit_cast(bf16x2, a), __builtin_bit_cast(bf16x2, b), c, false); }
DI float xhalf_max(float v) { auto r = __builtin_amdgcn_permlane32_swap(__float_as_uint(v), __float_as_uint(v), false, false); return fmaxf(__uint_as_float(r[0]), __uint_as_float(r[1])); }
DI float lo_bcast(float v) { auto r = __builtin_amdgcn_permlane32_swap(__float_as_uint(v), __float_as_uint(v), false, false); return __uint_as_float(r[0]); }

DI void glds16(const void* sbase, unsigned voff, unsigned lds_dst) {
    asm volatile("s_mov_b32 m0, %2\n\ts_nop 0\n\tglobal_load_lds_dwordx4 %0, %1" ::"v"(voff), "s"(sbase), "s"(lds_dst) : "memory");
}
DI void glds16x3(const void* sbase, unsigned v0, unsigned v1, unsigned v2, unsigned lds_dst) {
    asm volatile("s_mov_b32 m0, %4\n\ts_nop 0\n\tglobal_load_lds_dwordx4 %0, %3\n\tglobal_load_lds_dwordx4 %1, %3 offset:1024\n\tglobal_load_lds_dwordx4 %2, %3 offset:2048"
                 ::"v"(v0), "v"(v1), "v"(v2), "s"(sbase), "s"(lds_dst) : "memory");
}
DI void glds16x4(const void* sbase, const unsigned (&v)[4], unsigned lds_dst) {
    asm volatile("s_mov_b32 m0, %5\n\ts_nop 0\n\tglobal_load_lds_dwordx4 %0, %4\n\tglobal_load_lds_dwordx4 %1, %4 offset:1024\n\tglobal_load_lds_dwordx4 %2, %4 offset:2048\n\tglobal_load_lds_dwordx4 %3, %4 offset:3072"
                 ::"v"(v[0]), "v"(v[1]), "v"(v[2]), "v"(v[3]), "s"(sbase), "s"(lds_dst) : "memory");
}
template <int OFF> DI void gload16_sc1(bf16x8& dst, unsigned voff, const char* sbase) {
    asm volatile("global_load_dwordx4 %0, %1, %2 offset:%3 sc1" : "=v"(dst) : "v"(voff), "s"(sbase), "i"(OFF) : "memory");
}
DI void gload4_plain(unsigned& dst, unsigned voff, const void* sbase) {
    asm volatile("global_load_dword %0, %1, %2" : "+v"(dst) : "v"(voff), "s"(sbase) : "memory");
}
DI void gload4_sc1(unsigned& dst, unsigned voff, const void* sbase) {
    asm volatile("global_load_dword %0, %1, %2 sc1" : "=v"(dst) : "v"(voff), "s"(sbase) : "memory");
}
template <int N> DI void wait_vm_dep(bf16x8& v) { asm volatile("s_waitcnt vmcnt(%1)" : "+v"(v) : "i"(N)); }
template <int N> DI void wait_vm_dep2(bf16x8& v, bf16x8& w) { asm volatile("s_waitcnt vmcnt(%2)" : "+v"(v), "+v"(w) : "i"(N)); }
template <int N> DI void wait_vm_u(unsigned& v) { asm volatile("s_waitcnt vmcnt(%1)" : "+v"(v) : "i"(N)); }
template <int N> DI void wait_vm() { asm volatile("s_waitcnt vmcnt(%0)" ::"i"(N) : "memory"); }
#define WAIT_LGKM0() asm volatile("s_waitcnt lgkmcnt(0)" ::: "memory")
#define BAR() do { asm volatile("s_waitcnt lgkmcnt(0)" ::: "memory"); __builtin_amdgcn_s_barrier(); asm volatile("" ::: "memory"); } while (0)
#define BBAR() do { asm volatile("" ::: "memory"); __builtin_amdgcn_s_barrier(); asm volatile("" ::: "memory"); } while (0)
#define SB() __builtin_amdgcn_sched_barrier(0)
template <int... Is, class Fn> DI void sfor_impl(std::integer_sequence<int, Is...>, Fn&& f) { (f(std::integral_constant<int, Is>{}), ...); }
template <int N, class Fn> DI void sfor(Fn&& f) { sfor_impl(std::make_integer_sequence<int, N>{}, f); }
template <int V> using IC = std::integral_constant<int, V>;
DI void pin(bf16x8& v) { asm volatile("" : "+v"(v)); }
DI float glu_a(float g) { asm volatile("v_mul_f32 %0, 0xbfb8aa3b, %0\n\tv_exp_f32 %0, %0" : "+v"(g)); return g; }
DI float glu_b(float e, float u) { asm volatile("v_add_f32 %0, 1.0, %0\n\tv_rcp_f32 %0, %0\n\ts_nop 0\n\tv_mul_f32 %0, %0, %1" : "+v"(e) : "v"(u)); return e; }
template <int X, int ADD> DI unsigned xor_add(unsigned base) {
    unsigned r; asm volatile("v_xor_b32 %0, %2, %1\n\tv_add_u32 %0, %3, %0" : "=&v"(r) : "v"(base), "i"(X), "i"(ADD)); return r;
}
DI unsigned opaque(unsigned v) { asm volatile("" : "+v"(v)); return v; }
DI int fresh_lane() { int l; asm volatile("v_mbcnt_lo_u32_b32 %0, -1, 0\n\tv_mbcnt_hi_u32_b32 %0, -1, %0" : "=v"(l)); return l; }
DI bf16x8 tr_frag_o(unsigned base, int off0, int off1) {
    s16x4 lo = __builtin_amdgcn_ds_read_tr16_b64_v4i16((LAS s16x4*)((LAS char*)(size_t)base + off0));
    s16x4 hi = __builtin_amdgcn_ds_read_tr16_b64_v4i16((LAS s16x4*)((LAS char*)(size_t)base + off1));
    s16x8 f = __builtin_shufflevector(lo, hi, 0, 1, 2, 3, 4, 5, 6, 7);
    return __builtin_bit_cast(bf16x8, f);
}
DI u32x4 lds_rd128(unsigned a) { return *(LAS u32x4*)(size_t)a; }
DI unsigned lds_rd32(unsigned a) { return *(LAS unsigned*)(size_t)a; }
#define MFMA32(a, b, c) __builtin_amdgcn_mfma_f32_32x32x16_bf16((a), (b), (c), 0, 0, 0)
#define MFMA16(a, b, c) __builtin_amdgcn_mfma_f32_16x16x32_bf16((a), (b), (c), 0, 0, 0)

typedef __amdgpu_buffer_rsrc_t rsrc_t;
DI void st16_sc1(rsrc_t r, unsigned off, u32x4 v) { __builtin_amdgcn_raw_buffer_store_b128(v, r, off, 0, 16); }
DI void st16_sc1s(rsrc_t r, unsigned voff, unsigned soff, u32x4 v) {
    asm volatile("buffer_store_dwordx4 %0, %1, %2, %3 offen sc1\n\ts_nop 1" ::"v"(v), "v"(voff), "s"(r), "s"(soff) : "memory");
}
DI void st4_sc1(rsrc_t r, unsigned off, unsigned v) { __builtin_amdgcn_raw_buffer_store_b32(v, r, off, 0, 16); }
DI void flag_store(unsigned* sy, int idx, unsigned tag) { __hip_atomic_store((GAS unsigned*)sy + idx, tag, __ATOMIC_RELAXED, __HIP_MEMORY_SCOPE_AGENT); }
DI unsigned flag_load(const unsigned* sy, int idx) { return __hip_atomic_load((GAS unsigned*)sy + idx, __ATOMIC_RELAXED, __HIP_MEMORY_SCOPE_AGENT); }
constexpr unsigned kSpinCap = 1u << 15;
DI void wait_flags(const unsigned* sy, int idx, unsigned tag) {
    for (unsigned spins = 0; spins < kSpinCap; ++spins) {
        const unsigned v = flag_load(sy, idx);
        if (__all(v == tag)) break;
        __builtin_amdgcn_s_sleep(1);
    }
    asm volatile("" ::: "memory");
}

#define STAMP(i) do {} while (0)

template <int CTRL, int RMASK> DI float dpp_add(float v) {
    return v + __int_as_float(__builtin_amdgcn_update_dpp(0, __float_as_int(v), CTRL, RMASK, 0xf, true));
}
DI float wave_sum(float v) {
    v = dpp_add<0x111, 0xf>(v); v = dpp_add<0x112, 0xf>(v); v = dpp_add<0x114, 0xf>(v); v = dpp_add<0x118, 0xf>(v);
    v = dpp_add<0x142, 0xa>(v); v = dpp_add<0x143, 0xc>(v);
    return __int_as_float(__builtin_amdgcn_readlane(__float_as_int(v), 63));
}

constexpr int SLOT = 24576;
constexpr int SPARE = SLOT * 6;
constexpr int LDS_BYTES = 163840;
constexpr int KPOS = 4 * SLOT, VPOS = 5 * SLOT;
constexpr int WSL = 27648;
constexpr int WQ0 = 0, WQ1 = WSL, WQ2 = 2 * WSL, WQ3 = 3 * WSL, WQ4 = 4 * WSL, OTOP = 5 * WSL;
static_assert(WQ3 <= KPOS && OTOP + 18 * 1024 <= LDS_BYTES, "lds");

struct Ctx {
    const char* wq; const char* wgu;
    unsigned vq0, vq1, vq2, vg0, vg1, vg2;
    unsigned ldsbase;
    int wave; int P0;
    const char* wa; const char* wb; unsigned la, lb;
};
DI void issue_q(const Ctx& c, int chunk, int pos) { glds16x3(c.wq + (size_t)(chunk * 192) * 3456, c.vq0, c.vq1, c.vq2, c.ldsbase + pos * SLOT + c.wave * 3072); }
DI void issue_gu(const Ctx& c, int panel, int chunk, int pos) {
    glds16x3(c.wgu + (size_t)(chunk * 96) * 9216 + (size_t)(c.P0 + panel) * 128, c.vg0, c.vg1, c.vg2, c.ldsbase + pos * SLOT + c.wave * 3072);
}

constexpr int PF1 = 3;
DI void issue_slot(const Ctx& c, int m) {
    if (m < 6) issue_gu(c, 1, m, m % 6);
    else if (m < 9) issue_q(c, m - 6, m % 6);
    else if (m < 15) issue_gu(c, 0, m - 9, m % 6);
    else issue_gu(c, 2, m - 15, m % 6);
}

struct Hand {
    unsigned* sy; unsigned tag; rsrc_t rs; int tt, j, c4, cgrp;
    const char* kb; const char* vb; const char* qb;
};

constexpr int NSTEPS = 396;
struct Step { short slot, ks, t, set, tau, first, fmt; };
struct Sched {
    Step st[NSTEPS];
    short fill[NSTEPS + 100][3];
    short nrows;
    short qn;
    short qent[24][5];
    short qlast;
};
template <int C4> constexpr Sched make_sched() {
    Sched S{};
    const int NTa = 4 - C4, NTc = 1 + C4;
    int setsA[4] = {2, 3, 0, 1};
    int setsC[4] = {0, 0, 0, 0};
    {
        int k = 0;
        for (int s = 0; s < 4; ++s) { int order[4] = {3, 0, 1, 2}; int cand = order[s]; bool used = false; for (int t = 0; t < NTa; ++t) used |= (setsA[t] == cand); if (!used && k < NTc - 1) setsC[k++] = cand; }
        setsC[NTc - 1] = setsA[0];
    }
    int n = 0, lastStep[11] = {};
    for (int slot = 0; slot < 21; ++slot) {
        int pass = slot < 6 ? 0 : slot < 9 ? 1 : slot < 15 ? 2 : 3;
        int i = pass == 0 ? slot : pass == 1 ? slot - 6 : pass == 2 ? slot - 9 : slot - 15;
        int nt = pass == 0 ? 4 : pass == 1 ? 2 : pass == 2 ? NTa : NTc;
        int kps = pass == 1 ? 12 : 6;
        int nsl = pass == 1 ? 3 : 6;
        int tau0 = pass == 0 ? 0 : pass == 1 ? 4 : pass == 2 ? 6 : 6 + NTa;
        bool tmajor = (i == nsl - 1) || (i == 0 && pass > 0);
        for (int k = 0; k < nt * kps; ++k) {
            int t = tmajor ? k / kps : k % nt, s = tmajor ? k % kps : k / nt;
            Step e{};
            e.slot = slot; e.ks = i * kps + s; e.t = t; e.tau = tau0 + t; e.fmt = pass == 1; e.first = (e.ks == 0);
            e.set = pass == 0 ? t : pass == 1 ? t : pass == 2 ? setsA[t] : setsC[t];
            S.st[n] = e; lastStep[e.tau] = n; ++n;
        }
    }
    const int delta = C4 == 0 ? 0 : C4 == 1 ? 6 : C4 == 2 ? 4 : 2;
    int qn = 0;
    for (int sl = 0; sl < 3; ++sl)
        for (int t = 0; t < 2; ++t)
            for (int r = 0; r < 16; r += 2) {
                int m = 0, d0 = 0;
                for (int hh = 1; hh >= 0; --hh) { int nn = 32 * t + (r & 3) + 8 * (r >> 2) + 4 * hh - delta; if (nn >= 0 && nn < 54 && nn / 18 == sl) { m |= 1 << hh; d0 = nn % 18 - 4 * hh; } }
                if (m) { S.qent[qn][0] = t; S.qent[qn][1] = r; S.qent[qn][2] = sl; S.qent[qn][3] = m; S.qent[qn][4] = d0; ++qn; }
            }
    S.qn = qn;
    int freeg = 0;
    auto put = [&](int g, int code) { for (int k = 0; k < 3; ++k) if (S.fill[g][k] == 0) { S.fill[g][k] = code; return; } S.fill[g][0] = -1; };
    for (int tau = 0; tau < 11; ++tau) {
        if (tau == 4) continue;
        int ready = lastStep[tau] + 3;
        int g = ready > freeg ? ready : freeg;
        int dl = 100000;
        for (int k = lastStep[tau] + 1; k < NSTEPS; ++k) if (S.st[k].first && (S.st[k].set == S.st[lastStep[tau]].set || (tau == 5 && S.st[k].set == S.st[lastStep[4]].set))) { dl = k; break; }
        int ops[40] = {}, cnt = 0;
        if (tau == 5) {
            for (int sl = 0; sl < 3; ++sl) {
                for (int e = 0; e < qn; ++e) if (S.qent[e][2] == sl) ops[cnt++] = 2000 + e;
                ops[cnt++] = 2100 + sl; ops[cnt++] = 2110 + sl;
            }
        } else {
            const int b = 1000 + tau * 32;
            ops[cnt++] = b + 0;
            for (int j = 0; j < 7; ++j) { ops[cnt++] = b + j + 1; ops[cnt++] = b + 8 + j; }
            ops[cnt++] = b + 15; ops[cnt++] = b + 16; ops[cnt++] = b + 17;
        }
        int avail = dl - g, dbl = cnt > avail ? cnt - avail : 0;
        if (tau == 5 && dbl < cnt / 2) dbl = cnt / 2;
        int k = 0;
        while (k < cnt) { put(g, ops[k++]); if (dbl > 0 && k < cnt) { put(g, ops[k++]); --dbl; } ++g; }
        if (tau == 5) { for (int q = 0; q < NSTEPS + 100; ++q) for (int z = 0; z < 3; ++z) if (S.fill[q][z] == 2112) S.qlast = q; }
        freeg = g;
    }
    S.nrows = freeg;
    return S;
}
template <int C4> struct SchedOf { static constexpr Sched SC = make_sched<C4>(); };
template <int C4> constexpr int set_of(int tau) { for (int g = 0; g < NSTEPS; ++g) if (SchedOf<C4>::SC.st[g].tau == tau) return SchedOf<C4>::SC.st[g].set; return 0; }
constexpr int xpos(int s) { return s < 6 ? -1 : 4 * ((s - 6) / 3) + 2; }
constexpr int dpos(int d) {
    return d == 2 ? -1 : d == 3 ? 4 * 3 + 1 : d == 4 ? 4 * 9 + 1 : d == 5 ? 4 * 15 + 1 : d == 6 ? 4 * 21 + 1 : d == 7 ? 4 * 27 + 1 : d == 8 ? 24 * 5 + 1
         : d == 9 || d == 10 ? 24 * 6 + 1 : d == 11 || d == 12 ? 24 * 7 + 1 : 24 * (d - 5) + 1;
}
constexpr bool d_after(int e, int d) { return dpos(e) > dpos(d) || (dpos(e) == dpos(d) && e > d); }
constexpr int younger_than_x(int s, int now) {
    int n = 0;
    for (int t = s + 1; t < 36; ++t) if (xpos(t) < now) ++n;
    for (int d = 2; d <= 20; ++d) if (dpos(d) < now && (dpos(d) > xpos(s) || (dpos(d) == xpos(s) && d == 2))) n += 3;
    return n;
}
constexpr int younger_than_d(int d, int now) {
    int n = 0;
    for (int t = 0; t < 36; ++t) if (xpos(t) < now && xpos(t) > dpos(d)) ++n;
    for (int e = 2; e <= 20; ++e) if (dpos(e) < now && d_after(e, d)) n += 3;
    return n;
}

template <int C4>
DI void gemm1(const Ctx& c, const Hand& hd, char* ws, int tile32) {
#define S (SchedOf<C4>::SC)
    constexpr int NTa = 4 - C4;
    constexpr int NK = (C4 <= 1) ? 17 : (C4 == 2 ? 18 : 19), NV = (C4 <= 2) ? 18 : 19;
    constexpr int NCHK = C4 == 0 ? 14 : C4 == 1 ? 15 : C4 == 2 ? 17 : 18;
    constexpr int NQF = S.st[S.qlast < NSTEPS ? S.qlast : NSTEPS - 1].slot + 2;
    static_assert(NQF <= 13 && NQF >= 9, "qkv publish");
    unsigned Lgu, Lq = 0, l16;
    {
        const int lane = fresh_lane();
        const int hh = lane >> 5, blk = (lane >> 4) & 1, q = (lane & 15) >> 2, pp = lane & 3;
        Lgu = c.ldsbase + (unsigned)(blk * 12288 + (8 * hh + q) * 128 + 8 * pp + 32 * (((q >> 1) & 1) | (blk << 1)));
        l16 = lane * 16;
    }
    const unsigned hbs = (unsigned)WS_H + ((unsigned)tile32 * 288u + (unsigned)c.P0 * 4u) * 1024u;
    const unsigned stg = c.ldsbase + SPARE + c.wave * 2048;
    const unsigned qds = (unsigned)WS_QKV + (unsigned)((hd.cgrp * 3) * 2048 + tile32 * 32) * 48u;
    unsigned stw = 0;
    const char* xs = ws + WS_X + (size_t)tile32 * 36 * 1024;
    bf16x8 xf[36];
    f32x16 acc[4];
    unsigned cur[4];
    bf16x8 A[PF1 + 1];
    unsigned qflag = 0, pl0 = 0, pl1 = 0, warm0 = 0, warm1 = 0;
    sfor<6>([&](auto SI) { constexpr int s = SI; gload16_sc1<(s & 3) * 1024>(xf[s], l16, xs + (s >> 2) * 4096); });
    issue_slot(c, 2);
    auto rd = [&](auto IDX) {
        constexpr Step e = S.st[IDX];
        constexpr int kl = e.fmt ? e.ks % 12 : e.ks % 6, pos = e.slot % 6;
        constexpr int col = e.fmt ? e.t : (e.slot >= 9 && e.slot < 15 ? C4 + e.t : e.t);
        if constexpr (e.fmt && e.ks == 0 && e.t == 0) {
            const int lane = fresh_lane();
            const int hh = lane >> 5, blk = (lane >> 4) & 1, q = (lane & 15) >> 2, pp = lane & 3;
            Lq = c.ldsbase + (unsigned)((8 * hh + q) * 128 + 8 * pp + 32 * (blk ^ (((q >> 1) & 1) << 1)));
        }
        if constexpr (kl == 0) cur[e.t] = e.fmt ? xor_add<(col << 6), pos * SLOT>(Lq) : xor_add<(col << 5), pos * SLOT>(Lgu);
        return tr_frag_o(cur[e.t], kl * 2048, kl * 2048 + 512);
    };
    auto filler = [&](auto CODE) {
        constexpr int code = CODE;
        if constexpr (code >= 1000 && code < 2000) {
            constexpr int tau = (code - 1000) / 32, j = (code - 1000) % 32;
            constexpr int realset = set_of<C4>(tau);
            constexpr int fgo = tau < 4 ? 4 + tau : (tau < 6 + NTa ? C4 + tau - 6 : 8 + tau - 6 - NTa);
            if constexpr (j < 8) acc[realset][j] = glu_a(acc[realset][j]);
            else if constexpr (j < 16) acc[realset][j - 8] = glu_b(acc[realset][j - 8], acc[realset][j]);
            else if constexpr (j == 16) {
                pl0 = pk2(acc[realset][0], acc[realset][1]);
                pl1 = pk2(acc[realset][2], acc[realset][3]);
            } else {
                u32x4 w = {pl0, pl1, pk2(acc[realset][4], acc[realset][5]), pk2(acc[realset][6], acc[realset][7])};
                st16_sc1s(hd.rs, l16, hbs + fgo * 1024, w);
            }
        } else if constexpr (code >= 2000 && code < 2100) {
            constexpr int e = code - 2000, t = S.qent[e][0], r = S.qent[e][1], m = S.qent[e][3], d0 = S.qent[e][4];
            if constexpr (e == 0) { const int ln = fresh_lane(); stw = opaque(stg + (ln & 31) * 48 + ((ln >> 5) << 3)); }
            const float qs = (hd.cgrp * 3 + S.qent[e][2] < 32) ? 0.23570226039551584f * 1.4426950408889634f : 1.f;
            const unsigned v = pk2(acc[t][r] * qs, acc[t][r + 1] * qs);
            if constexpr (m == 3) *(LAS unsigned*)(size_t)(stw + 2 * d0) = v;
            else if ((int)((stw >> 3) & 1) == (m >> 1)) *(LAS unsigned*)(size_t)(stw + 2 * d0) = v;
        } else if constexpr (code >= 2100 && code < 2110) {
            constexpr int sl = code - 2100;
            if (((stw >> 3) & 1) == 0) *(LAS unsigned*)(size_t)(stw + 36) = (hd.cgrp * 3 + sl >= 64) ? 0x00003F80u : 0u;
            else *(LAS u32x2*)(size_t)(stw + 32) = (u32x2){0u, 0u};
        } else if constexpr (code >= 2110) {
            constexpr int sl = code - 2110;
            const u32x4 w0 = lds_rd128(stg + l16);
            st16_sc1s(hd.rs, l16, qds + (unsigned)sl * 2048u * 48u, w0);
            if (fresh_lane() < 32) { const u32x4 w1 = lds_rd128(stg + 1024 + l16); st16_sc1s(hd.rs, l16, qds + (unsigned)sl * 2048u * 48u + 1024, w1); }
        }
    };
    sfor<NSTEPS>([&](auto IDX) {
        constexpr int idx = IDX;
        constexpr Step e = S.st[idx];
        constexpr bool top = (idx == 0) || (S.st[idx > 0 ? idx - 1 : 0].slot != e.slot);
        if constexpr (top) {
            constexpr int n = e.slot;
            if constexpr (n == 5) {
                wait_vm<(younger_than_x(35, 120) < younger_than_d(6, 120) ? younger_than_x(35, 120) : younger_than_d(6, 120))>();
                pin(xf[30]); pin(xf[31]); pin(xf[32]); pin(xf[33]); pin(xf[34]); pin(xf[35]);
            } else if constexpr (n == NQF) wait_vm<3>();
            else if constexpr (n >= 1 && n <= 16) wait_vm<younger_than_d(n + 1, 24 * n)>();
            else if constexpr (n == 17) wait_vm<6>();
            else if constexpr (n == 18) wait_vm<3 + (NK <= 17 ? 3 : 0)>();
            else if constexpr (n == 19) wait_vm<(NK <= 18 ? 3 : 0) + (NV <= 18 ? 5 : 0)>();
            if constexpr (n == 7) {
                asm volatile("" ::"v"(warm0));
                const unsigned o = (unsigned)c.wave * 8192u + (unsigned)fresh_lane() * 128u;
                if (blockIdx.x < 92) gload4_plain(warm1, o < c.lb ? o : c.lb, c.wb);
            }
            if constexpr (n == 12) asm volatile("" ::"v"(warm1));
            if constexpr (n == NCHK) {
                if (c.wave == 0) {
                    wait_vm_u<(n + 4 <= 20 ? 3 : 0)>(qflag);
                    if (!__all(qflag == hd.tag)) { const int ln = fresh_lane(); wait_flags(hd.sy, SY_QF + ((ln >= 32 && hd.tt > 0) ? hd.tt - 1 : hd.tt) * 64 + (ln & 31), hd.tag); }
                    asm volatile("buffer_inv sc1" ::: "memory");
                }
            }
            if constexpr (n == NK) { if (c.wave == 0) wait_vm<(NCHK + 5 <= 20 ? 3 * (20 - (NCHK + 5) + 1) : 0)>(); }
            if constexpr (n > 0) BBAR();
            if constexpr (n == NQF) { if (c.wave == 0 && fresh_lane() == 0) flag_store(hd.sy, SY_QF + hd.tt * 64 + hd.cgrp, hd.tag); }
            if constexpr (n == NCHK - 1) { if (c.wave == 0) { const int lane = fresh_lane(); gload4_sc1(qflag, (unsigned)(SY_QF + ((lane >= 32 && hd.tt > 0) ? hd.tt - 1 : hd.tt) * 64 + (lane & 31)) * 4u, hd.sy); } }
            sfor<19>([&](auto DD) { constexpr int d = DD + 2; if constexpr (dpos(d) == 24 * n + 1 && n >= 5) issue_slot(c, d); });
            if constexpr (n == NK || n == NV) {
#pragma unroll
                for (int kv = 0; kv < 2; ++kv) {
                    if ((kv == 0 && n == NK) || (kv == 1 && n == NV)) {
#pragma unroll
                        for (int k = 0; k < 3; ++k) {
                            const int m = c.wave * 3 + k;
                            int off = (hd.tt * 256 - 256) * 48 + m * 1024;
                            if (off < 0) off = m * 1024;
                            glds16((kv == 0 ? hd.kb : hd.vb) + off, l16, c.ldsbase + (kv == 0 ? KPOS : VPOS) + m * 1024);
                        }
                    }
                }
            }
            if constexpr (n == NV) {
#pragma unroll
                for (int k = 0; k < 2; ++k) {
                    int m = c.wave * 2 + k; if (m > 11) m = 11;
                    glds16(hd.qb + m * 1024, l16, c.ldsbase + SPARE + m * 1024);
                }
            }
            if constexpr (idx == 0) sfor<PF1>([&](auto J) { A[J] = rd(J); });
        }
        if constexpr (idx + PF1 < NSTEPS) A[(idx + PF1) % (PF1 + 1)] = rd(IC<idx + PF1>{});
        if constexpr (e.slot < 5 && e.t == 0) {
            sfor<6>([&](auto DD) { constexpr int d = DD + 3; if constexpr (d <= 7 && dpos(d) == 4 * e.ks + 1) issue_slot(c, d); });
            sfor<3>([&](auto JJ) { constexpr int sx = 6 + 3 * e.ks + JJ; if constexpr (sx < 36) gload16_sc1<(sx & 3) * 1024>(xf[sx], l16, xs + (sx >> 2) * 4096); });
            if constexpr (e.ks == 10) {
                const unsigned o = (unsigned)c.wave * 8192u + (unsigned)fresh_lane() * 128u;
                if (blockIdx.x < 193) gload4_plain(warm0, o < c.la ? o : c.la, c.wa);
            }
            wait_vm_dep<younger_than_x(e.ks, 4 * e.ks + 3)>(xf[e.ks]);
        }
        SB();
        if constexpr (e.first) {
            f32x16 z;
#pragma unroll
            for (int r = 0; r < 16; ++r) z[r] = 0.f;
            acc[e.set] = MFMA32(A[idx % (PF1 + 1)], xf[e.ks], z);
        } else acc[e.set] = MFMA32(A[idx % (PF1 + 1)], xf[e.ks], acc[e.set]);
        SB();
        if constexpr (S.fill[idx][0] != 0) {
            filler(IC<S.fill[idx][0]>{});
            if constexpr (S.fill[idx][1] != 0) filler(IC<S.fill[idx][1]>{});
            if constexpr (S.fill[idx][2] != 0) filler(IC<S.fill[idx][2]>{});
            SB();
        }
    });
    sfor<S.nrows - NSTEPS>([&](auto R) {
        constexpr int g = NSTEPS + R;
        if constexpr (S.fill[g][0] != 0) filler(IC<S.fill[g][0]>{});
        if constexpr (S.fill[g][1] != 0) filler(IC<S.fill[g][1]>{});
        if constexpr (S.fill[g][2] != 0) filler(IC<S.fill[g][2]>{});
    });
}
#undef S

constexpr int g2_ks(int u) { constexpr int o[18] = {0, 1, 2, 3, 5, 6, 7, 9, 10, 11, 14, 15, 4, 8, 12, 13, 16, 17}; return o[u]; }
constexpr int g2_place(int sl) { return sl == 0 ? WQ0 : sl == 1 ? WQ1 : sl == 2 ? WQ2 : sl == 3 ? WQ3 : sl == 4 ? WQ4 : WQ0; }
constexpr int ewait(int u) { constexpr int c[8] = {0, 4, 8, 16, 16, 20, 24, 24}; return 2 * (11 - u) + 2 + c[u]; }

__global__ __launch_bounds__(512, 2) void mega_kernel(const u16* __restrict__ x, const u16* __restrict__ wqkv, const u16* __restrict__ wo,
                                                      const u16* __restrict__ wg, const u16* __restrict__ wu, const u16* __restrict__ wd,
                                                      char* __restrict__ ws, u16* __restrict__ out) {
    extern __shared__ __attribute__((aligned(16))) char smem[];
    const int tid = threadIdx.x;
    const int wave = __builtin_amdgcn_readfirstlane(tid >> 6);
    const int bid = blockIdx.x, xj = bid & 7, loc = bid >> 3;
    const int c4 = loc & 3, tt = loc >> 2, cgrp = xj * 4 + c4;
    const unsigned ldsbase = (unsigned)(size_t)(LAS char*)smem;
    unsigned* sy = (unsigned*)(ws + WS_SYNC);
    const rsrc_t rs = __builtin_amdgcn_make_buffer_rsrc(ws, 0, (unsigned)WS_USED, 0x00020000);
    unsigned tag;
    Ctx c;

    STAMP(0);
    {
        const int lane = tid & 63;
        unsigned cwarm = 0;
        if (wave == 7) {
            const char* pc;
            asm volatile("s_getpc_b64 %0" : "=s"(pc));
            gload4_plain(cwarm, (unsigned)((loc * 24 + (lane < 24 ? lane : 23)) * 128), pc);
        }
        const int xrow = bid * 8 + wave;
        const char* xr = (const char*)x + (size_t)xrow * 1152;
        u32x4 xv0 = *(const u32x4*)(xr + lane * 16);
        u32x4 xv1 = *(const u32x4*)(xr + 1024 + (lane & 7) * 16);
        const unsigned gen = flag_load(sy, SY_GEN);
        const int qa0 = (108 * cgrp) & ~15;
        c.wq = (const char*)wqkv + qa0; c.wgu = (const char*)((wave >> 2) ? wu : wg);
        c.ldsbase = ldsbase; c.wave = wave; c.P0 = (9 * cgrp) >> 2;
        {
            const int lrow = lane >> 3, c16 = lane & 7, bsel = wave >> 2;
            const int swgu = ((lrow >> 1) & 1) | (bsel << 1);
            const int chgu = (((c16 >> 1) ^ swgu) << 1) | (c16 & 1);
            c.vg0 = (unsigned)((24 * (wave & 3) + lrow) * 9216 + chgu * 16); c.vg1 = c.vg0 + 8 * 9216 - 1024; c.vg2 = c.vg0 + 16 * 9216 - 2048;
            const int swq = ((lrow >> 1) & 1) << 1;
            int chq = (((c16 >> 1) ^ swq) << 1) | (c16 & 1);
            if (cgrp == 31 && chq == 7) chq = 6;
            c.vq0 = (unsigned)((24 * wave + lrow) * 3456 + chq * 16); c.vq1 = c.vq0 + 8 * 3456 - 1024; c.vq2 = c.vq0 + 16 * 3456 - 2048;
        }
        tag = (unsigned)__builtin_amdgcn_readfirstlane((int)(gen * 0x9E3779B1u + 0x7F4A7C15u));
        if (bid < 81) { c.wa = (const char*)wg + (size_t)bid * 65536; c.la = 65536 - 128; }
        else if (bid < 162) { c.wa = (const char*)wu + (size_t)(bid - 81) * 65536; c.la = 65536 - 128; }
        else { const int k = (bid - 162) % 31; c.wa = (const char*)wqkv + (size_t)k * 65536; c.la = (k == 30) ? 1990656 - 30 * 65536 - 128 : 65536 - 128; }
        if (bid < 81) { c.wb = (const char*)wd + (size_t)bid * 65536; c.lb = 65536 - 128; }
        else { const int k = (bid - 81) % 11; c.wb = (const char*)wo + (size_t)k * 65536; c.lb = (k == 10) ? 663552 - 10 * 65536 - 128 : 65536 - 128; }
        float s1 = 0.f;
        if (lane >= 8) xv1 = (u32x4){0u, 0u, 0u, 0u};
#pragma unroll
        for (int k = 0; k < 4; ++k) { s1 = dot2(xv0[k], 0x3f803f80u, s1); s1 = dot2(xv1[k], 0x3f803f80u, s1); }
        asm volatile("" ::"v"(cwarm));
        const float meanf = wave_sum(s1) * (1.f / 576.f);
        float s2 = 0.f;
#pragma unroll
        for (int k = 0; k < 4; ++k) {
            float a = lo2f(xv0[k]) - meanf, b = hi2f(xv0[k]) - meanf; s2 += a * a + b * b;
            if (lane < 8) { a = lo2f(xv1[k]) - meanf; b = hi2f(xv1[k]) - meanf; s2 += a * a + b * b; }
        }
        const float var = rbf(wave_sum(s2) * (1.f / 576.f));
        const float mean = rbf(meanf);
        const float rden = 1.f / rbf(sqrtf(rbf(var + 1e-5f)));
        u32x4 o0, o1;
#pragma unroll
        for (int k = 0; k < 4; ++k) {
            o0[k] = pk2(rbf(lo2f(xv0[k]) - mean) * rden, rbf(hi2f(xv0[k]) - mean) * rden);
            o1[k] = pk2(rbf(lo2f(xv1[k]) - mean) * rden, rbf(hi2f(xv1[k]) - mean) * rden);
        }
        *(LAS u32x4*)(size_t)(ldsbase + SPARE + (lane * 8 + wave) * 16) = o0;
        if (lane < 8) *(LAS u32x4*)(size_t)(ldsbase + SPARE + ((64 + lane) * 8 + wave) * 16) = o1;
        BAR();
        const unsigned xd = (unsigned)WS_X + (unsigned)(bid >> 2) * 36u * 1024u;
        const int t0 = (bid & 3) * 8;
        {
            const int i = tid, cc = i >> 3, tk = i & 7;
            st16_sc1(rs, xd + ((cc >> 1) * 64 + (cc & 1) * 32 + t0 + tk) * 16, lds_rd128(ldsbase + SPARE + i * 16));
        }
        {
            const int i = 512 + lane, cc = i >> 3, tk = i & 7;
            st16_sc1(rs, wave == 0 ? xd + ((cc >> 1) * 64 + (cc & 1) * 32 + t0 + tk) * 16 : 0xFFFFFF00u, lds_rd128(ldsbase + SPARE + i * 16));
        }
        issue_slot(c, 0); issue_slot(c, 1);
        wait_vm<6>();
        BBAR();
        if (tid == 0) { flag_store(sy, SY_XF + (bid >> 5) * 64 + (bid & 31), tag); flag_store(sy, SY_START + bid, tag); }
    }
    STAMP(1);
    Hand hd;
    hd.sy = sy; hd.tag = tag; hd.rs = rs; hd.tt = tt; hd.j = xj; hd.c4 = c4; hd.cgrp = cgrp;
    hd.kb = ws + WS_QKV + ((size_t)(32 + cgrp) * 2048) * 48; hd.vb = ws + WS_QKV + ((size_t)(64 + cgrp) * 2048) * 48;
    hd.qb = ws + WS_QKV + ((size_t)cgrp * 2048 + tt * 256) * 48;

    if (wave == 0) wait_flags(sy, SY_XF + tt * 64 + (fresh_lane() & 31), tag);
    wait_vm<0>();
    BBAR();
    STAMP(2);
    const int tile32 = tt * 8 + wave;
    if (c4 == 0) gemm1<0>(c, hd, ws, tile32);
    else if (c4 == 1) gemm1<1>(c, hd, ws, tile32);
    else if (c4 == 2) gemm1<2>(c, hd, ws, tile32);
    else gemm1<3>(c, hd, ws, tile32);
    STAMP(7);

    {
    const int lane = fresh_lane();
    const int hh = lane >> 5, blk = (lane >> 4) & 1, q = (lane & 15) >> 2, pp = lane & 3;
    const unsigned l16 = lane * 16;
    const bool tid0 = (wave == 0) && (lane == 0);
    const char* wdb = (const char*)wd + (size_t)(xj * 576) * 1152 + c4 * 288;
    const int mb = wave < 6 ? 4 * wave : 23;
    unsigned vo[4];
#pragma unroll
    for (int i = 0; i < 4; ++i) {
        const int n = (mb + i) * 64 + lane, row = n / 18, cc = n - row * 18;
        vo[i] = (unsigned)(row * 1152 + cc * 16 - 1024 * i);
    }
    auto issue_wd = [&](int slot, int dst) { glds16x4(wdb + (size_t)slot * 96 * 1152, vo, ldsbase + dst + mb * 1024); };
    const int q4 = lane >> 4, t16 = lane & 15, qq = (lane & 15) >> 2;
    const char* hsrc = ws + WS_H + ((size_t)tile32 * 288 + xj * 36) * 1024;
    const unsigned hvo = (unsigned)(((q4 >> 1) * 64 + (q4 & 1) * 32 + t16) * 16);
    bf16x8 hf[18][2];
    auto hload = [&](auto UI) { constexpr int u = UI, s = g2_ks(u);
        gload16_sc1<(s & 1) * 2048>(hf[u][0], hvo, hsrc + (s >> 1) * 4096);
        gload16_sc1<(s & 1) * 2048 + 256>(hf[u][1], hvo, hsrc + (s >> 1) * 4096); };
    unsigned hflag = 0;
    const int hfi = (xj * 8 + tt) * 64;
    {
        if (c4 == 0) wait_vm<1>(); else if (c4 == 1) wait_vm<2>(); else if (c4 == 2) wait_vm<3>(); else wait_vm<4>();
        BBAR();
        if (tid0) flag_store(sy, SY_HF + hfi + c4, tag);
        issue_wd(0, WQ0); issue_wd(1, WQ1); issue_wd(2, WQ2);
        STAMP(8);
        const int t = lane & 31;
        const int q0 = tt * 256 + wave * 32;
        bf16x8 Qf0, Qf1;
        {
            const unsigned qa = ldsbase + SPARE + (wave * 32 + t) * 48;
            Qf0 = __builtin_bit_cast(bf16x8, lds_rd128(qa + 16 * hh));
            const unsigned qv1 = lds_rd32(qa + 32);
            u32x4 q1 = {hh == 0 ? qv1 : 0u, 0u, 0u, 0u};
            Qf1 = __builtin_bit_cast(bf16x8, q1);
        }
        const unsigned kaddr = ldsbase + KPOS + (wave * 32 + t) * 48;
        const unsigned vaddr = ldsbase + VPOS + (wave * 32 + 4 * hh + q) * 48 + 32 * blk + 8 * pp;
        const float NEG = -__builtin_inff();
        f32x16 o;
        auto attend = [&](auto FULLC) {
            constexpr bool FULL = FULLC;
            const int ktmin = FULL ? 0 : 8 - wave;
            f32x16 sc[9];
#pragma unroll
            for (int kt = 0; kt < 9; ++kt) {
                if (FULL || kt >= ktmin) {
                    u32x4 k0 = lds_rd128(kaddr + kt * 1536 + 16 * hh);
                    u32x4 k1 = {lds_rd32(kaddr + kt * 1536 + 32), 0u, 0u, 0u};
                    f32x16 z;
#pragma unroll
                    for (int r = 0; r < 16; ++r) z[r] = 0.f;
                    z = MFMA32(__builtin_bit_cast(bf16x8, k0), Qf0, z);
                    sc[kt] = MFMA32(__builtin_bit_cast(bf16x8, k1), Qf1, z);
                } else {
#pragma unroll
                    for (int r = 0; r < 16; ++r) sc[kt][r] = NEG;
                }
            }
#pragma unroll
            for (int r = 0; r < 16; ++r) {
                const int kk = (r & 3) + 8 * (r >> 2) + 4 * hh;
                if (!(kk > t)) sc[0][r] = NEG;
                if (!(kk <= t)) sc[8][r] = NEG;
            }
            float m = NEG;
#pragma unroll
            for (int kt = 0; kt < 9; ++kt)
#pragma unroll
                for (int r = 0; r < 16; ++r) m = fmaxf(m, sc[kt][r]);
            m = xhalf_max(m);
            if (wave == 0) gload4_sc1(hflag, (unsigned)(SY_HF + hfi + (lane & 3)) * 4u, sy);
#pragma unroll
            for (int r = 0; r < 16; ++r) o[r] = 0.f;
            sfor<9>([&](auto KT) {
                constexpr int kt = KT;
                if constexpr (kt == 2) {
                    wait_vm<12>();
                    if (wave == 0) {
                        wait_vm_u<0>(hflag);
                        if (!__all(hflag == tag)) wait_flags(sy, SY_HF + hfi + (lane & 3), tag);
                    }
                    BBAR();
                    if (tid0) flag_store(sy, SY_H2 + hfi + c4, tag);
                }
                if constexpr (kt == 6) { if (wave == 0) gload4_sc1(hflag, (unsigned)(SY_H2 + hfi + (lane & 3)) * 4u, sy); }
                if (FULL || kt >= ktmin) {
                    unsigned pk[8];
#pragma unroll
                    for (int r = 0; r < 16; r += 2)
                        pk[r >> 1] = pk2(__builtin_amdgcn_exp2f(sc[kt][r] - m), __builtin_amdgcn_exp2f(sc[kt][r + 1] - m));
#pragma unroll
                    for (int s = 0; s < 2; ++s) {
                        u32x4 pb = {pk[4 * s], pk[4 * s + 1], pk[4 * s + 2], pk[4 * s + 3]};
                        bf16x8 A = tr_frag_o(vaddr, (kt * 32 + 16 * s) * 48, (kt * 32 + 16 * s + 8) * 48);
                        o = MFMA32(A, __builtin_bit_cast(bf16x8, pb), o);
                    }
                }
                if constexpr (kt >= 3) { SB(); hload(IC<2 * (kt - 3)>{}); hload(IC<2 * (kt - 3) + 1>{}); SB(); }
            });
        };
        if (tt > 0) attend(std::true_type{}); else attend(std::false_type{});
        const float inv = 1.f / lo_bcast(o[10]);
        const int tile16 = (q0 >> 4) + ((lane >> 4) & 1);
        const unsigned ob = (unsigned)WS_O + (unsigned)(tile16 * 18) * 1024u + t16 * 16;
        u32x4 w = {pk2(o[0] * inv, o[1] * inv), pk2(o[2] * inv, o[3] * inv), pk2(o[4] * inv, o[5] * inv), pk2(o[6] * inv, o[7] * inv)};
        st16_sc1(rs, ob + (cgrp >> 1) * 1024 + (2 * (cgrp & 1) + hh) * 256, w);
        st4_sc1(rs, hh == 0 ? ob + (16 + (cgrp >> 4)) * 1024 + ((cgrp >> 2) & 3) * 256 + (cgrp & 3) * 4 : 0xFFFFFFF0u, pk2(o[8] * inv, o[9] * inv));
        if (wave == 0) {
            wait_vm_u<14>(hflag);
            if (!__all(hflag == tag)) wait_flags(sy, SY_H2 + hfi + (lane & 3), tag);
        }
    }
    STAMP(9);
    f32x4 acc[2][9];
    const unsigned arel = ldsbase + (unsigned)((16 * (q4 >> 1) + 4 * (q4 & 1) + qq) * 288 + 8 * pp);
    const unsigned pdu = (unsigned)WS_P + (unsigned)(((tt * 4 + c4) * 8 + wave) * 8 + xj) * 9u * 1024u;
    const char* wob = (const char*)wo + c4 * 288;
    unsigned vw[4];
    auto wo_offsets = [&](bool last) {
        const int ln = fresh_lane();
#pragma unroll
        for (int i = 0; i < 4; ++i) {
            const int n = (mb + i) * 64 + ln, rs_ = n / 18, cc = n - rs_ * 18;
            const int u1 = rs_ >> 5, r1 = rs_ & 31, qx = 2 * (r1 >> 4) + ((r1 >> 2) & 1), tx = (r1 >> 3) & 1, qy = r1 & 3;
            const int f = 18 * (qx >> 1) + 4 * (qx & 1) + 8 * tx + qy;
            const int jx = 4 * tx + qy, g = 4 * (u1 - 1) + qx;
            const int row5 = (u1 == 0) ? 36 * 15 + f : 18 * (4 * g + (jx >> 1)) + 16 + (jx & 1);
            vw[i] = (unsigned)((last ? row5 : 36 * u1 + f) * 1152 + cc * 16 - 1024 * i);
        }
    };
    auto issue_wo = [&](int slot, int dst) { glds16x4(slot < 5 ? wob + (size_t)slot * 108 * 1152 : wob, vw, ldsbase + dst + mb * 1024); };
    {
        wait_vm<26>();
        BAR();
        constexpr int PF = 4;
        bf16x8 A[PF + 1];
        unsigned oflag = 0;
        constexpr int NS0 = 126, NST = 162;
        auto st_u = [](int idx) constexpr { return idx < NS0 ? idx / 9 : 14 + (idx - NS0) % 4; };
        auto st_dt = [](int idx) constexpr { return idx < NS0 ? idx % 9 : (idx - NS0) / 4; };
        unsigned cur = 0, curl = 0;
        auto rd = [&](auto IDX) {
            constexpr int idx = IDX, u = st_u(idx), dt = st_dt(idx);
            constexpr int ks = g2_ks(u), off = g2_place(ks / 3) + (ks % 3) * 32 * 288;
            if constexpr (idx < NS0) {
                if constexpr (dt == 0) cur = xor_add<0, off>(arel);
                return tr_frag_o(cur, dt * 32, dt * 32 + 8 * 288);
            } else {
                if constexpr (idx == NS0) { cur = xor_add<0, 0>(arel); curl = xor_add<0, WQ4>(arel); }
                if constexpr (off >= WQ4) return tr_frag_o(curl, off - WQ4 + dt * 32, off - WQ4 + dt * 32 + 8 * 288);
                else return tr_frag_o(cur, off + dt * 32, off + dt * 32 + 8 * 288);
            }
        };
        sfor<NST>([&](auto IDX) {
            constexpr int idx = IDX, u = st_u(idx), dt = st_dt(idx);
            if constexpr (idx < NS0 && dt == 0) {
                if constexpr (u == 0) STAMP(10);
                if constexpr (u == 3) { BBAR(); issue_wd(5, WQ0); }
                if constexpr (u == 6) {
                    wait_vm<12>();
                    sfor<6>([&](auto K) { pin(hf[6 + K][0]); pin(hf[6 + K][1]); });
                    BBAR();
                    if (tid0) flag_store(sy, SY_OF + tt * 64 + cgrp, tag);
                }
                if constexpr (u == 8) { if (wave == 0) gload4_sc1(oflag, (unsigned)(SY_OF + tt * 64 + (lane & 31)) * 4u, sy); }
                if constexpr (u == 10) {
                    wait_vm<8>();
                    if (wave == 0) {
                        wait_vm_u<0>(oflag);
                        if (!__all(oflag == tag)) wait_flags(sy, SY_OF + tt * 64 + (lane & 31), tag);
                        asm volatile("buffer_inv sc1" ::: "memory");
                    }
                    BBAR();
                }
                if constexpr (u == 12) {
                    if (wave == 0) wait_vm<1>(); else wait_vm<0>();
                    sfor<6>([&](auto K) { pin(hf[12 + K][0]); pin(hf[12 + K][1]); });
                    wo_offsets(false);
                }
                if constexpr (idx == 0) sfor<PF>([&](auto J) { A[J] = rd(J); });
                if constexpr (u < 6) wait_vm_dep2<ewait(u)>(hf[u][0], hf[u][1]);
            }
            if constexpr (idx == NS0) {
                if (wave == 0) wait_vm<0>();
                BBAR();
                const char* osrc = ws + WS_O + (size_t)((tt * 8 + xj) * 2) * 18 * 1024;
#pragma unroll
                for (int i = 0; i < 5; ++i) {
                    int m = wave + 8 * i; if (m >= 36) m -= 8;
                    glds16(osrc + m * 1024, l16, ldsbase + (m < 18 ? OTOP + m * 1024 : WQ3 + (m - 18) * 1024));
                }
                issue_wo(0, WQ1); issue_wo(1, WQ2);
            }
            if constexpr (idx + PF < NST) A[(idx + PF) % (PF + 1)] = rd(IC<idx + PF>{});
            SB();
            if constexpr (u == 0) {
                acc[0][dt] = MFMA16(A[idx % (PF + 1)], hf[u][0], ((f32x4){0.f, 0.f, 0.f, 0.f}));
                acc[1][dt] = MFMA16(A[idx % (PF + 1)], hf[u][1], ((f32x4){0.f, 0.f, 0.f, 0.f}));
            } else {
                acc[0][dt] = MFMA16(A[idx % (PF + 1)], hf[u][0], acc[0][dt]);
                acc[1][dt] = MFMA16(A[idx % (PF + 1)], hf[u][1], acc[1][dt]);
            }
            SB();
            if constexpr (idx < NS0 && dt == 2) {
                if constexpr (u == 0) issue_wd(3, WQ3);
                if constexpr (u == 1) { hload(IC<12>{}); hload(IC<13>{}); }
                if constexpr (u == 2) issue_wd(4, WQ4);
                if constexpr (u == 4) { hload(IC<14>{}); hload(IC<15>{}); }
                if constexpr (u == 5) { hload(IC<16>{}); hload(IC<17>{}); }
                SB();
            }
            if constexpr (idx >= NS0 && u == 16 && dt > 0) {
                constexpr int d = dt - 1;
                u32x4 w = {pk2(acc[0][d][0], acc[0][d][1]), pk2(acc[0][d][2], acc[0][d][3]), pk2(acc[1][d][0], acc[1][d][1]), pk2(acc[1][d][2], acc[1][d][3])};
                st16_sc1s(rs, l16, pdu + d * 1024, w);
                SB();
            }
        });
        STAMP(16);
        u32x4 w = {pk2(acc[0][8][0], acc[0][8][1]), pk2(acc[0][8][2], acc[0][8][3]), pk2(acc[1][8][0], acc[1][8][1]), pk2(acc[1][8][2], acc[1][8][3])};
        st16_sc1s(rs, l16, pdu + 8 * 1024, w);
    }
    {
        wait_vm<0>();
        BAR();
        if (tid0) flag_store(sy, SY_PF + (tt * 4 + c4) * 64 + xj, tag);
        STAMP(17);
        issue_wo(2, WQ0); issue_wo(3, WQ4);
        const int tok0 = tt * 256 + xj * 32;
        const unsigned psrc = (unsigned)WS_P + (unsigned)(((tt * 4 + c4) * 8 + xj) * 8) * 9u * 1024u + l16;
        auto fin = [&](auto NDC) {
            constexpr int ND = NDC;
            u32x2 xv[ND][2];
            u32x4 vy[ND][8];
            f32x4 sacc[ND][2];
            auto sgroup = [&](auto G) {
                constexpr int g = G;
                bf16x8 B0[3], B1[3], A[3][ND];
                auto rdk = [&](auto KK) {
                    constexpr int ks = 6 * g + KK, sl = ks / 3;
                    constexpr int place = sl == 0 ? WQ1 : sl == 1 ? WQ2 : sl == 2 ? WQ0 : sl == 3 ? WQ4 : sl == 4 ? WQ1 : WQ2;
                    B0[KK % 3] = __builtin_bit_cast(bf16x8, lds_rd128(ldsbase + OTOP + ks * 1024 + l16));
                    B1[KK % 3] = __builtin_bit_cast(bf16x8, lds_rd128(ldsbase + WQ3 + ks * 1024 + l16));
#pragma unroll
                    for (int k = 0; k < ND; ++k) A[KK % 3][k] = tr_frag_o(arel + place + (ks % 3) * 32 * 288 + ((k == 0) ? wave : 8) * 32, 0, 8 * 288);
                };
                rdk(IC<0>{}); rdk(IC<1>{});
                sfor<6>([&](auto KK) {
                    constexpr int kk = KK, ks = 6 * g + kk;
                    if constexpr (kk + 2 < 6) rdk(IC<kk + 2>{});
                    SB();
#pragma unroll
                    for (int k = 0; k < ND; ++k) {
                        if constexpr (ks == 0) { sacc[k][0] = MFMA16(A[kk % 3][k], B0[kk % 3], ((f32x4){0.f, 0.f, 0.f, 0.f})); sacc[k][1] = MFMA16(A[kk % 3][k], B1[kk % 3], ((f32x4){0.f, 0.f, 0.f, 0.f})); }
                        else { sacc[k][0] = MFMA16(A[kk % 3][k], B0[kk % 3], sacc[k][0]); sacc[k][1] = MFMA16(A[kk % 3][k], B1[kk % 3], sacc[k][1]); }
                    }
                    SB();
                });
            };
            sgroup(IC<0>{});
            BAR();
            glds16x4(wob + (size_t)4 * 108 * 1152, vw, ldsbase + WQ1 + mb * 1024);
            wo_offsets(true);
            issue_wo(5, WQ2);
#pragma unroll
            for (int k = 0; k < ND; ++k) {
                const int dt = (k == 0) ? wave : 8;
#pragma unroll
                for (int a = 0; a < 2; ++a)
                    xv[k][a] = *(const u32x2*)((const char*)x + (size_t)(tok0 + a * 16 + t16) * 1152 + (c4 * 144 + dt * 16 + 4 * q4) * 2);
            }
            wait_flags(sy, SY_PF + (tt * 4 + c4) * 64 + (lane & 7), tag);
            STAMP(18);
#pragma unroll
            for (int k = 0; k < ND; ++k) {
                const int dt = (k == 0) ? wave : 8;
#pragma unroll
                for (int sj = 0; sj < 8; ++sj) vy[k][sj] = __builtin_amdgcn_raw_buffer_load_b128(rs, psrc + (sj * 9 + dt) * 1024, 0, 16);
            }
            BBAR();
            sgroup(IC<1>{});
            sgroup(IC<2>{});
#pragma unroll
            for (int k = 0; k < ND; ++k) {
                const int dt = (k == 0) ? wave : 8;
                float y[8];
#pragma unroll
                for (int e = 0; e < 8; ++e) y[e] = 0.f;
#pragma unroll
                for (int sj = 0; sj < 8; ++sj)
#pragma unroll
                    for (int e = 0; e < 4; ++e) { y[2 * e] += lo2f(vy[k][sj][e]); y[2 * e + 1] += hi2f(vy[k][sj][e]); }
#pragma unroll
                for (int a = 0; a < 2; ++a) {
                    float ov[4];
#pragma unroll
                    for (int r = 0; r < 4; ++r) {
                        const float yy = rbf(y[4 * a + r]);
                        const float g = rbf(yy * __builtin_amdgcn_rcpf(1.f + __builtin_amdgcn_exp2f(-yy * 1.4426950408889634f)));
                        const float pr = rbf(rbf(sacc[k][a][r]) * g);
                        const unsigned xw = xv[k][a][r >> 1];
                        ov[r] = ((r & 1) ? hi2f(xw) : lo2f(xw)) + pr;
                    }
                    u32x2 w = {pk2(ov[0], ov[1]), pk2(ov[2], ov[3])};
                    *(u32x2*)((char*)out + (size_t)(tok0 + a * 16 + t16) * 1152 + (c4 * 144 + dt * 16 + 4 * q4) * 2) = w;
                }
            }
        };
        if (wave == 0) fin(IC<2>{}); else fin(IC<1>{});
        if (bid == 0 && wave == 7) {
            for (unsigned spins = 0; spins < kSpinCap; ++spins) {
                bool ok = true;
#pragma unroll
                for (int k = 0; k < 4; ++k) ok &= flag_load(sy, SY_START + lane + 64 * k) == tag;
                if (__all(ok)) break;
                __builtin_amdgcn_s_sleep(1);
            }
            if (lane == 0) flag_store(sy, SY_GEN, tag);
        }
    }
    }
    STAMP(19);
}

extern "C" void kernel_launch(void* const* d_in, const int* in_sizes, int n_in, void* d_out, int out_size, void* d_ws, size_t ws_size,
                              hipStream_t stream) {
    const u16* x = (const u16*)d_in[0];
    const u16* wqkv = (const u16*)d_in[1];
    const u16* wo = (const u16*)d_in[2];
    const u16* wg = (const u16*)d_in[4];
    const u16* wu = (const u16*)d_in[5];
    const u16* wd = (const u16*)d_in[6];
    (void)hipFuncSetAttribute((const void*)mega_kernel, hipFuncAttributeMaxDynamicSharedMemorySize, LDS_BYTES);
    mega_kernel<<<256, 512, LDS_BYTES, stream>>>(x, wqkv, wo, wg, wu, wd, (char*)d_ws, (u16*)d_out);
}
```
